```python
import torch

def _softmax(x, axis=-1):
    return torch.softmax(x, dim=axis)
def _gelu(x):
    return torch.nn.functional.gelu(x, approximate="tanh")
def _silu(x):
    return torch.nn.functional.silu(x)
def _sigmoid(x):
    return torch.sigmoid(x)
def _layernorm(x, axis=-1, eps=1e-5):
    m = x.mean(dim=axis, keepdim=True)
    v = x.var(dim=axis, keepdim=True, unbiased=False)
    return (x - m) / torch.sqrt(v + eps)
def _scatter_add(tgt, idx, src, axis=0):
    return tgt.clone().index_add_(axis, idx, src)
def _segment_sum(src, seg, n):
    out = torch.zeros((n,) + src.shape[1:], dtype=src.dtype)
    return out.index_add_(0, seg, src)
def _pad(x, pw):
    flat = tuple(v for pair in reversed(pw) for v in pair)
    return torch.nn.functional.pad(x, flat)

kernel_name = 'standalone__sliding_block__prompt'

def setup_inputs():

    _w = (lambda a: (lambda t: t.to(torch.bfloat16) if t.is_floating_point() else t)(torch.as_tensor(a)))
    B, S, D = 1, 2048, 512
    return {
        "x": _w((lambda *s: torch.randn(*s, dtype=torch.bfloat16) * 0.4)(B, S, D)),
        "wqkv": _w((lambda *s: torch.randn(*s, dtype=torch.bfloat16) * 0.4)(D, 3 * D)),
        "wo": _w((lambda *s: torch.randn(*s, dtype=torch.bfloat16) * 0.05)(D, D)),
    }

def reference(x, wqkv, wo):
    B, S, D = x.shape
    H = 32; Dh = D // H; W = 256
    qkv = (lambda x, *d: x.permute(*d))((x @ wqkv).reshape(B, S, 3, H, Dh), 2, 0, 3, 1, 4)
    q, k, v = qkv[0], qkv[1], qkv[2]
    scores = torch.einsum("bhqd,bhkd->bhqk", q, k) * (Dh ** -0.5)
    idx = torch.arange(S)
    mask = (idx[:, None] - idx[None, :] >= 0) & (idx[:, None] - idx[None, :] < W)
    scores = torch.where(mask[None, None], scores, -1e9)
    p = _softmax(scores, axis=-1)
    o = torch.einsum("bhqk,bhkd->bhqd", p, v)
    return (lambda x, *d: x.permute(*d))(o, 0, 2, 1, 3).reshape(B, S, D) @ wo
```

```cpp
#include <hip/hip_runtime.h>
#include <hip/hip_bf16.h>
#include <stdint.h>

typedef short bf16x8 __attribute__((ext_vector_type(8)));
typedef float f32x16 __attribute__((ext_vector_type(16)));
typedef unsigned short u16;

#define ROUND_SCORES 0

__device__ __forceinline__ u16 f2bf(float x) {
    __hip_bfloat16 b = __float2bfloat16(x);
    return *reinterpret_cast<u16*>(&b);
}
__device__ __forceinline__ float bf2f(u16 x) { return __uint_as_float(((unsigned)x) << 16); }

__global__ __launch_bounds__(64) void gemm_simple(const u16* A, const u16* B, u16* C, int M, int N, int K) {
    int lane = threadIdx.x, r = lane & 31, h = lane >> 5;
    int bm = blockIdx.y * 32, bn = blockIdx.x * 32;
    f32x16 acc = {};
    for (int k0 = 0; k0 < K; k0 += 16) {
        bf16x8 a = *reinterpret_cast<const bf16x8*>(A + (size_t)(bm + r) * K + k0 + 8 * h);
        bf16x8 b;
        for (int j = 0; j < 8; ++j) b[j] = (short)B[(size_t)(k0 + 8 * h + j) * N + bn + r];
        acc = __builtin_amdgcn_mfma_f32_32x32x16_bf16(a, b, acc, 0, 0, 0);
    }
    for (int i = 0; i < 16; ++i) {
        int row = (i & 3) + 8 * (i >> 2) + 4 * h;
        C[(size_t)(bm + row) * N + bn + r] = f2bf(acc[i]);
    }
}

__global__ void attn_simple(const u16* qkv, u16* o) {
    int idx = blockIdx.x * blockDim.x + threadIdx.x;
    int q = idx & 2047, hd = idx >> 11;
    float qv[16];
    for (int d = 0; d < 16; ++d) qv[d] = bf2f(qkv[(size_t)q * 1536 + hd * 16 + d]);
    int lo = q - 255; if (lo < 0) lo = 0;
    float m = -1e30f;
    for (int j = lo; j <= q; ++j) {
        float s = 0;
        for (int d = 0; d < 16; ++d) s += qv[d] * bf2f(qkv[(size_t)j * 1536 + 512 + hd * 16 + d]);
        m = fmaxf(m, s);
    }
    float l = 0; float ov[16] = {};
    for (int j = lo; j <= q; ++j) {
        float s = 0;
        for (int d = 0; d < 16; ++d) s += qv[d] * bf2f(qkv[(size_t)j * 1536 + 512 + hd * 16 + d]);
        float p = __expf((s - m) * 0.25f);
        l += p;
        float pb = bf2f(f2bf(p));
        for (int d = 0; d < 16; ++d) ov[d] += pb * bf2f(qkv[(size_t)j * 1536 + 1024 + hd * 16 + d]);
    }
    for (int d = 0; d < 16; ++d) o[(size_t)q * 512 + hd * 16 + d] = f2bf(ov[d] / l);
}

extern "C" void kernel_launch(void* const* d_in, const int* in_sizes, int n_in,
                              void* d_out, int out_size, void* d_ws, size_t ws_size,
                              hipStream_t stream) {
    const u16* x = (const u16*)d_in[0];
    const u16* wqkv = (const u16*)d_in[1];
    const u16* wo = (const u16*)d_in[2];
    u16* qkv = (u16*)d_ws;
    u16* o = qkv + 2048 * 1536;
    gemm_simple<<<dim3(1536 / 32, 2048 / 32), 64, 0, stream>>>(x, wqkv, qkv, 2048, 1536, 512);
    attn_simple<<<2048 * 32 / 256, 256, 0, stream>>>(qkv, o);
    gemm_simple<<<dim3(512 / 32, 2048 / 32), 64, 0, stream>>>(o, wo, (u16*)d_out, 2048, 512, 512);
}
```

```cpp
#include <hip/hip_runtime.h>
#include <stdint.h>

typedef short bf16x8 __attribute__((ext_vector_type(8)));
typedef short s16x4 __attribute__((ext_vector_type(4)));
typedef float f32x16 __attribute__((ext_vector_type(16)));
typedef float f32x4 __attribute__((ext_vector_type(4)));
typedef float f32x2 __attribute__((ext_vector_type(2)));
typedef unsigned u32x4 __attribute__((ext_vector_type(4)));
typedef unsigned u32x2 __attribute__((ext_vector_type(2)));
typedef __bf16 bf16x2 __attribute__((ext_vector_type(2)));
typedef unsigned short u16;
#define LAS __attribute__((address_space(3)))
#define GAS __attribute__((address_space(1)))
#define DI __device__ __forceinline__

constexpr unsigned WS_EPOCH = 0;
constexpr unsigned WS_START = 1024;
constexpr unsigned WS_KV = 4096;
constexpr unsigned WS_O = WS_KV + 16 * 16 * 16384;
constexpr unsigned WS_END = WS_O + 64 * 32 * 1024;
constexpr int SLOT = 28672;
constexpr int L_K = 3 * SLOT;
constexpr int L_V = L_K + 24576;
constexpr int L_Q = L_V + 24576;
constexpr int L_ONES = 5 * SLOT;
constexpr int L_WO = 0;
constexpr int L_OST = 65536;
constexpr int LDS_BYTES = L_ONES + 1024;

constexpr float CEXP = 0.36067376022224085f;
constexpr unsigned SENT = 0xFFFFFFFFu;
constexpr int SPIN_CAP = 1 << 18;

DI float round_bf16(float s) {
    f32x2 v = {0.0f, s};
    return __builtin_bit_cast(float, __builtin_convertvector(v, bf16x2));
}
DI unsigned pack2(float a, float b) {
    f32x2 v = {a, b};
    return __builtin_bit_cast(unsigned, __builtin_convertvector(v, bf16x2));
}
DI u32x4 pack8(const f32x16& a, int o) {
    u32x4 r = {pack2(a[o], a[o + 1]), pack2(a[o + 2], a[o + 3]), pack2(a[o + 4], a[o + 5]), pack2(a[o + 6], a[o + 7])};
    return r;
}
DI u32x4 sanitize(u32x4 v) {
    u32x4 r = {min(v.x, SENT - 1), min(v.y, SENT - 1), min(v.z, SENT - 1), min(v.w, SENT - 1)};
    return r;
}
DI bf16x8 as_bf(u32x4 v) { return __builtin_bit_cast(bf16x8, v); }
DI void bar() { asm volatile("s_waitcnt lgkmcnt(0)" ::: "memory"); __builtin_amdgcn_s_barrier(); asm volatile("" ::: "memory"); }
#define WAITV(n) asm volatile("s_waitcnt vmcnt(" #n ")" ::: "memory")
#define MFMA32(a, b, c) __builtin_amdgcn_mfma_f32_32x32x16_bf16((a), (b), (c), 0, 0, 0)
#define MFMA16(a, b, c) __builtin_amdgcn_mfma_f32_16x16x32_bf16((a), (b), (c), 0, 0, 0)
DI s16x4 trread(LAS char* p) { return __builtin_amdgcn_ds_read_tr16_b64_v4i16((LAS s16x4*)p); }
DI bf16x8 tr2(LAS char* p0, LAS char* p1) {
    s16x4 a = trread(p0), b = trread(p1);
    return __builtin_shufflevector(a, b, 0, 1, 2, 3, 4, 5, 6, 7);
}
DI void glds16(const void* g, LAS char* l) {
    __builtin_amdgcn_global_load_lds((const GAS unsigned*)g, (LAS unsigned*)l, 16, 0, 0);
}
DI float xhalf_max(float m) {
    auto r = __builtin_amdgcn_permlane32_swap(__float_as_uint(m), __float_as_uint(m), false, false);
    return fmaxf(__uint_as_float(r[0]), __uint_as_float(r[1]));
}
#define STAMP(i) do {} while (0)
#define CSTAMP(i) do {} while (0)

__global__ __launch_bounds__(512, 2) void swa_block(const u16* __restrict__ x, const u16* __restrict__ wqkv,
                                                    const u16* __restrict__ wo, u16* __restrict__ out, char* ws) {
    extern __shared__ __attribute__((aligned(16))) char smem_[];
    LAS char* lds = (LAS char*)smem_;
    const int tid = threadIdx.x;
    const int wave = __builtin_amdgcn_readfirstlane(tid >> 6);
    const int lane = tid & 63, r32 = lane & 31, hh = lane >> 5;
    const int bid = blockIdx.x, g = bid & 7, ii = bid >> 3;
    const int hp = (g & 1) * 8 + (ii & 7);
    const int ck = (g >> 1) * 4 + (ii >> 3);
    const __amdgpu_buffer_rsrc_t wsr = __builtin_amdgcn_make_buffer_rsrc(ws, 0, WS_END, 0x00020000);
    const unsigned kvbase = WS_KV + (unsigned)(hp * 16 + ck) * 16384u;
    STAMP(0); CSTAMP(14);

    if (wave >= 4) {
        const int lw = wave - 4;
        const u16* xs[2];
        {
            const int c0 = (lane & 7) ^ (lane >> 4);
            const u16* xb = x + (size_t)(ck * 128 + lw * 32 + (lane >> 3)) * 512;
            xs[0] = xb + c0 * 8;
            xs[1] = xb + (c0 ^ 4) * 8;
        }
        const u16* wsrc = wqkv + (size_t)(lw * 16 + (lane >> 2)) * 1536 + hp * 32 + (lane & 3) * 8;
        auto issue = [&](int st, int slot) {
            LAS char* sb = lds + slot * SLOT;
#pragma unroll
            for (int i = 0; i < 4; ++i) glds16(xs[i & 1] + i * 8 * 512 + st * 64, sb + (lw * 32 + i * 8) * 128);
            const u16* wp = wsrc + (size_t)st * 64 * 1536;
            glds16(wp + 512, sb + 16384 + lw * 1024);
            glds16(wp + 1024, sb + 16384 + 4096 + lw * 1024);
            glds16(wp, sb + 16384 + 8192 + lw * 1024);
        };
        issue(0, 0); issue(1, 1); issue(2, 2); issue(3, 3); issue(4, 4);
        if (lw == 0) { u32x4 one = {0x3F803F80u, 0x3F803F80u, 0x3F803F80u, 0x3F803F80u}; *(LAS u32x4*)(lds + L_ONES + lane * 16) = one; }
        WAITV(28); bar();
        WAITV(21); bar(); issue(5, 0);
        WAITV(21); bar(); issue(6, 1);
        WAITV(21); bar(); issue(7, 2);
        WAITV(21); bar();
        WAITV(14); bar();
        WAITV(7); bar();
        WAITV(0); bar();
    } else {
        unsigned epoch = __builtin_amdgcn_raw_buffer_load_b32(wsr, WS_EPOCH, 0, 16);
        {
            const u32x4 sv = {SENT, SENT, SENT, SENT};
#pragma unroll
            for (int i = 0; i < 4; ++i) __builtin_amdgcn_raw_buffer_store_b128(sv, wsr, kvbase + (wave * 4 + i) * 1024 + lane * 16, 0, 16);
#pragma unroll
            for (int i = 0; i < 2; ++i)
                __builtin_amdgcn_raw_buffer_store_b128(sv, wsr, WS_O + (unsigned)((ck * 4 + wave) * 32 + hp * 2 + i) * 1024 + lane * 16, 0, 16);
        }
        const unsigned tag = __builtin_amdgcn_readfirstlane(epoch) + 1u;
        int xoff[4];
        {
            const int u = hh ^ ((r32 >> 1) & 7);
#pragma unroll
            for (int ks = 0; ks < 4; ++ks) xoff[ks] = (wave * 32 + r32) * 128 + (((2 * ks) ^ u) << 4);
        }
        const int woff = 16384 + (8 * hh + ((lane & 15) >> 2)) * 64 + ((lane >> 4) & 1) * 32 + (lane & 3) * 8;
        f32x16 aK = {}, aV = {}, aQ = {};
        WAITV(0);
        u32x4 sflags = {0, 0, 0, 0};
        for (int st = 0; st < 8; ++st) {
            bar();
            if (st == 0) { STAMP(1); if (tid == 0) __builtin_amdgcn_raw_buffer_store_b32(tag, wsr, WS_START + bid * 4, 0, 16); }
            if (st == 6 && wave == 1) sflags = __builtin_amdgcn_raw_buffer_load_b128(wsr, WS_START + lane * 16, 0, 16);
            LAS char* sb = lds + (st >= 5 ? st - 5 : st) * SLOT;
#pragma unroll
            for (int ks = 0; ks < 4; ++ks) {
                bf16x8 xf = *(LAS bf16x8*)(sb + xoff[ks]);
                LAS char* wp = sb + woff + ks * 1024;
                bf16x8 wk = tr2(wp, wp + 256);
                bf16x8 wv = tr2(wp + 4096, wp + 4096 + 256);
                bf16x8 wq = tr2(wp + 8192, wp + 8192 + 256);
                aK = MFMA32(wk, xf, aK);
                aV = MFMA32(xf, wv, aV);
                aQ = MFMA32(wq, xf, aQ);
            }
        }
        STAMP(2);
        const u32x4 k0 = sanitize(pack8(aK, 0)), k1 = sanitize(pack8(aK, 8));
        const u32x4 v0 = sanitize(pack8(aV, 0)), v1 = sanitize(pack8(aV, 8));
        const int vo = (r32 >> 4) * 4096 + wave * 1024 + hh * 256 + (r32 & 15) * 16;
        __builtin_amdgcn_raw_buffer_store_b128(k0, wsr, kvbase + wave * 1024 + lane * 16, 0, 16);
        __builtin_amdgcn_raw_buffer_store_b128(k1, wsr, kvbase + 4096 + wave * 1024 + lane * 16, 0, 16);
        __builtin_amdgcn_raw_buffer_store_b128(v0, wsr, kvbase + 8192 + vo, 0, 16);
        __builtin_amdgcn_raw_buffer_store_b128(v1, wsr, kvbase + 8192 + vo + 512, 0, 16);
        *(LAS u32x4*)(lds + L_K + (8 + wave) * 1024 + lane * 16) = k0;
        *(LAS u32x4*)(lds + L_K + 12288 + (8 + wave) * 1024 + lane * 16) = k1;
        const int vl = (r32 >> 4) * 12288 + (8 + wave) * 1024 + hh * 256 + (r32 & 15) * 16;
        *(LAS u32x4*)(lds + L_V + vl) = v0;
        *(LAS u32x4*)(lds + L_V + vl + 512) = v1;
        *(LAS u32x4*)(lds + L_Q + wave * 1024 + lane * 16) = pack8(aQ, 0);
        *(LAS u32x4*)(lds + L_Q + 4096 + wave * 1024 + lane * 16) = pack8(aQ, 8);
        if (wave == 1) {
            for (int spin = 0; spin < SPIN_CAP; ++spin) {
                const bool ok = sflags.x == tag && sflags.y == tag && sflags.z == tag && sflags.w == tag;
                if (__all(ok)) break;
                __builtin_amdgcn_s_sleep(1);
                asm volatile("" ::: "memory");
                sflags = __builtin_amdgcn_raw_buffer_load_b128(wsr, WS_START + lane * 16, 0, 16);
            }
            if (bid == 0 && lane == 0) __builtin_amdgcn_raw_buffer_store_b32(tag, wsr, WS_EPOCH, 0, 16);
        }
    }
    bar();
    STAMP(3);

    const int hd = wave & 1;
    const int qb = (wave < 4) ? 3 - (wave >> 1) : ((wave - 4) >> 1);
    const int orow = g * 256 + (ii >> 3) * 64, ocol = (ii & 7) * 64;
    auto issue_wo = [&]() {
        const int krow = lane >> 3, s16 = lane & 7;
        const int src16 = (((s16 >> 1) ^ ((krow >> 1) & 3)) << 1) | (s16 & 1);
        const u16* p = wo + (size_t)(wave * 64 + krow) * 512 + ocol + src16 * 8;
#pragma unroll
        for (int i = 0; i < 8; ++i) glds16(p + (size_t)i * 8 * 512, lds + L_WO + (wave * 64 + i * 8) * 128);
    };
    if (wave == 4 || wave == 5) {
#pragma unroll
        for (int cc = 0; cc < 2; ++cc) {
            const int c2 = ck - 1 - cc;
            if (c2 >= 0) {
                const unsigned src = WS_KV + (unsigned)(hp * 16 + c2) * 16384u + hd * 4096 + lane * 16;
                u32x4 d[8];
                for (int spin = 0; spin < SPIN_CAP; ++spin) {
                    asm volatile("" ::: "memory");
#pragma unroll
                    for (int i = 0; i < 8; ++i) d[i] = __builtin_amdgcn_raw_buffer_load_b128(wsr, src + (i >> 2) * 8192 + (i & 3) * 1024, 0, 16);
                    unsigned mx = 0;
#pragma unroll
                    for (int i = 0; i < 8; ++i) mx = max(max(mx, max(d[i].x, d[i].y)), max(d[i].z, d[i].w));
                    if (__all(mx != SENT)) break;
                }
#pragma unroll
                for (int i = 0; i < 8; ++i)
                    *(LAS u32x4*)(lds + ((i >> 2) ? L_V : L_K) + hd * 12288 + ((1 - cc) * 4 + (i & 3)) * 1024 + lane * 16) = d[i];
            }
        }
        STAMP(4);
    }
    issue_wo();

    const bf16x8 qf = *(LAS bf16x8*)(lds + L_Q + hd * 4096 + qb * 1024 + lane * 16);
    LAS char* kp = lds + L_K + hd * 12288 + qb * 1024 + lane * 16;
    const int va0 = (r32 < 16) ? (L_V + hd * 12288 + qb * 1024 + hh * 256 + r32 * 16) : (L_ONES + (r32 - 16) * 16);
    const int vstr = (r32 < 16) ? 1024 : 0;
    const int tmin = (ck >= 2) ? 1 : max(1, 8 - 4 * ck - qb);
    const bool has_first = ck >= 2;
    f32x16 O = {};
    float mrun = -__builtin_inff();
    for (int grp = 0; grp < 2; ++grp) {
        const int lo = grp ? tmin : 8 - qb, hi = grp ? 8 - qb : 8;
        if (grp) { bar(); STAMP(5); }
        f32x16 S[9];
        bf16x8 kf[9];
#pragma unroll
        for (int t = 0; t < 9; ++t) kf[t] = *(LAS bf16x8*)(kp + t * 1024);
        const f32x16 zero = {};
#pragma unroll
        for (int t = 1; t < 8; ++t) if (t >= lo && t < hi) S[t] = MFMA32(kf[t], qf, zero);
        if (grp) {
            S[8] = MFMA32(kf[8], qf, zero);
            if (has_first) S[0] = MFMA32(kf[0], qf, zero);
            else {
#pragma unroll
                for (int i = 0; i < 16; ++i) S[0][i] = -__builtin_inff();
            }
#pragma unroll
            for (int i = 0; i < 16; ++i) {
                const int key = (i & 3) + 8 * (i >> 2) + 4 * hh;
                S[0][i] = (key > r32) ? S[0][i] : S[8][i];
            }
        }
        float m = mrun;
#pragma unroll
        for (int t = 0; t < 8; ++t) if (t ? (t >= lo && t < hi) : (grp != 0)) {
#pragma unroll
            for (int i = 0; i < 16; i += 2) m = fmaxf(fmaxf(m, S[t][i]), S[t][i + 1]);
        }
        m = round_bf16(xhalf_max(m));
        if (grp) {
            const float alpha = __builtin_amdgcn_exp2f((mrun - m) * CEXP);
#pragma unroll
            for (int i = 0; i < 9; ++i) O[i] *= alpha;
        }
        mrun = m;
        const float nmc = -m * CEXP;
#pragma unroll
        for (int t = 1; t < 8; ++t) if (t >= lo && t < hi) {
            LAS char* vp = lds + va0 + t * vstr;
            const bf16x8 vf0 = *(LAS bf16x8*)(vp), vf1 = *(LAS bf16x8*)(vp + 512);
            f32x16 e;
#pragma unroll
            for (int i = 0; i < 16; ++i) e[i] = __builtin_amdgcn_exp2f(__builtin_fmaf(round_bf16(S[t][i]), CEXP, nmc));
            O = MFMA32(vf0, as_bf(pack8(e, 0)), O);
            O = MFMA32(vf1, as_bf(pack8(e, 8)), O);
        }
        if (grp) {
            f32x16 e;
#pragma unroll
            for (int i = 0; i < 16; ++i) e[i] = __builtin_amdgcn_exp2f(__builtin_fmaf(round_bf16(S[0][i]), CEXP, nmc));
            const u32x4 p0 = pack8(e, 0), p1 = pack8(e, 8);
            u32x4 mk0, mk1;
#pragma unroll
            for (int j = 0; j < 4; ++j) {
                const int ka = ((2 * j) & 3) + 8 * ((2 * j) >> 2) + 4 * hh;
                mk0[j] = ((ka > r32) ? 0xFFFFu : 0u) | ((ka + 1 > r32) ? 0xFFFF0000u : 0u);
                mk1[j] = ((ka + 16 > r32) ? 0xFFFFu : 0u) | ((ka + 17 > r32) ? 0xFFFF0000u : 0u);
            }
            {
                LAS char* vp = lds + va0 + 8 * vstr;
                const bf16x8 vf0 = *(LAS bf16x8*)(vp), vf1 = *(LAS bf16x8*)(vp + 512);
                O = MFMA32(vf0, as_bf(p0 & ~mk0), O);
                O = MFMA32(vf1, as_bf(p1 & ~mk1), O);
            }
            if (has_first) {
                LAS char* vp = lds + va0;
                const bf16x8 vf0 = *(LAS bf16x8*)(vp), vf1 = *(LAS bf16x8*)(vp + 512);
                O = MFMA32(vf0, as_bf(p0 & mk0), O);
                O = MFMA32(vf1, as_bf(p1 & mk1), O);
            }
        }
    }
    {
        const float rl = 1.0f / O[8];
        f32x16 on;
#pragma unroll
        for (int i = 0; i < 8; ++i) on[i] = O[i] * rl;
        const u32x4 ov = sanitize(pack8(on, 0));
        __builtin_amdgcn_raw_buffer_store_b128(ov, wsr, WS_O + (unsigned)((ck * 4 + qb) * 32 + hp * 2 + hd) * 1024 + lane * 16, 0, 16);
    }
    STAMP(6);
    WAITV(1);
    bar();
    STAMP(7);

    {
        const int ct = wave & 3, th = wave >> 2;
        const unsigned src = WS_O + (unsigned)((orow >> 5) * 32 + wave * 8) * 1024 + lane * 16;
        u32x4 d[8];
#pragma unroll
        for (int i = 0; i < 8; ++i) d[i] = __builtin_amdgcn_raw_buffer_load_b128(wsr, src + i * 1024, 0, 16);
        bf16x8 wf[16];
        {
            const int gq = lane >> 4, q = (lane & 15) >> 2, p = lane & 3;
            const int kr = 16 * (gq >> 1) + 4 * (gq & 1) + q;
            const int a0 = L_WO + kr * 128 + ((ct ^ ((kr >> 1) & 3)) << 5) + p * 8;
#pragma unroll
            for (int s = 0; s < 16; ++s) wf[s] = tr2(lds + a0 + s * 4096, lds + a0 + s * 4096 + 1024);
        }
        for (int spin = 0; spin < SPIN_CAP; ++spin) {
            unsigned mx = 0;
#pragma unroll
            for (int i = 0; i < 8; ++i) mx = max(max(mx, max(d[i].x, d[i].y)), max(d[i].z, d[i].w));
            if (__all(mx != SENT)) break;
            asm volatile("" ::: "memory");
#pragma unroll
            for (int i = 0; i < 8; ++i) d[i] = __builtin_amdgcn_raw_buffer_load_b128(wsr, src + i * 1024, 0, 16);
        }
        STAMP(8);
#pragma unroll
        for (int i = 0; i < 8; ++i) *(LAS u32x4*)(lds + L_OST + (wave * 8 + i) * 1024 + lane * 16) = d[i];
        bar();
        STAMP(9);
        f32x4 c0 = {}, c1 = {};
        const int gq = lane >> 4;
        LAS char* op = lds + L_OST + th * 32768 + (gq >> 1) * 1024 + (gq & 1) * 512 + (lane & 15) * 16;
#pragma unroll
        for (int s = 0; s < 16; ++s) {
            const bf16x8 o0 = *(LAS bf16x8*)(op + s * 2048), o1 = *(LAS bf16x8*)(op + s * 2048 + 256);
            c0 = MFMA16(wf[s], o0, c0);
            c1 = MFMA16(wf[s], o1, c1);
        }
        u16* dst = out + (size_t)(orow + th * 32 + (lane & 15)) * 512 + ocol + ct * 16 + gq * 4;
        u32x2 r0 = {pack2(c0[0], c0[1]), pack2(c0[2], c0[3])}, r1 = {pack2(c1[0], c1[1]), pack2(c1[2], c1[3])};
        *(u32x2*)dst = r0;
        *(u32x2*)(dst + 16 * 512) = r1;
        STAMP(10); CSTAMP(15);
    }
}

extern "C" void kernel_launch(void* const* d_in, const int* in_sizes, int n_in, void* d_out, int out_size, void* d_ws,
                              size_t ws_size, hipStream_t stream) {
    (void)in_sizes; (void)n_in; (void)out_size; (void)ws_size;
    (void)hipFuncSetAttribute((const void*)swa_block, hipFuncAttributeMaxDynamicSharedMemorySize, LDS_BYTES);
    swa_block<<<256, 512, LDS_BYTES, stream>>>((const u16*)d_in[0], (const u16*)d_in[1], (const u16*)d_in[2], (u16*)d_out, (char*)d_ws);
}
```

```cpp
#include <hip/hip_runtime.h>
#include <stdint.h>

typedef short bf16x8 __attribute__((ext_vector_type(8)));
typedef short s16x4 __attribute__((ext_vector_type(4)));
typedef float f32x16 __attribute__((ext_vector_type(16)));
typedef float f32x4 __attribute__((ext_vector_type(4)));
typedef float f32x2 __attribute__((ext_vector_type(2)));
typedef unsigned u32x4 __attribute__((ext_vector_type(4)));
typedef unsigned u32x2 __attribute__((ext_vector_type(2)));
typedef __bf16 bf16x2 __attribute__((ext_vector_type(2)));
typedef unsigned short u16;
#define LAS __attribute__((address_space(3)))
#define GAS __attribute__((address_space(1)))
#define DI __device__ __forceinline__

constexpr unsigned WS_EPOCH = 0;
constexpr unsigned WS_START = 1024;
constexpr unsigned WS_KV = 4096;
constexpr unsigned WS_O = WS_KV + 16 * 16 * 16384;
constexpr unsigned WS_END = WS_O + 64 * 32 * 1024;
constexpr int SLOT = 28672;
constexpr int L_K = 3 * SLOT;
constexpr int L_V = L_K + 24576;
constexpr int L_Q = L_V + 24576;
constexpr int L_ONES = 5 * SLOT;
constexpr int L_WO = 0;
constexpr int L_OST = 65536;
constexpr int LDS_BYTES = L_ONES + 1024;

constexpr float CEXP = 0.36067376022224085f;
constexpr unsigned SENT = 0xFFFFFFFFu;
constexpr int SPIN_CAP = 1 << 18;

DI float round_bf16(float s) {
    f32x2 v = {0.0f, s};
    return __builtin_bit_cast(float, __builtin_convertvector(v, bf16x2));
}
DI unsigned pack2(float a, float b) {
    f32x2 v = {a, b};
    return __builtin_bit_cast(unsigned, __builtin_convertvector(v, bf16x2));
}
DI u32x4 pack8(const f32x16& a, int o) {
    u32x4 r = {pack2(a[o], a[o + 1]), pack2(a[o + 2], a[o + 3]), pack2(a[o + 4], a[o + 5]), pack2(a[o + 6], a[o + 7])};
    return r;
}
DI u32x4 sanitize(u32x4 v) {
    u32x4 r = {min(v.x, SENT - 1), min(v.y, SENT - 1), min(v.z, SENT - 1), min(v.w, SENT - 1)};
    return r;
}
DI bf16x8 as_bf(u32x4 v) { return __builtin_bit_cast(bf16x8, v); }
DI void bar() { asm volatile("s_waitcnt lgkmcnt(0)" ::: "memory"); __builtin_amdgcn_s_barrier(); asm volatile("" ::: "memory"); }
#define WAITV(n) asm volatile("s_waitcnt vmcnt(" #n ")" ::: "memory")
#define MFMA32(a, b, c) __builtin_amdgcn_mfma_f32_32x32x16_bf16((a), (b), (c), 0, 0, 0)
#define MFMA16(a, b, c) __builtin_amdgcn_mfma_f32_16x16x32_bf16((a), (b), (c), 0, 0, 0)
DI s16x4 trread(LAS char* p) { return __builtin_amdgcn_ds_read_tr16_b64_v4i16((LAS s16x4*)p); }
DI bf16x8 tr2(LAS char* p0, LAS char* p1) {
    s16x4 a = trread(p0), b = trread(p1);
    return __builtin_shufflevector(a, b, 0, 1, 2, 3, 4, 5, 6, 7);
}
DI void glds16(const void* g, LAS char* l) {
    __builtin_amdgcn_global_load_lds((const GAS unsigned*)g, (LAS unsigned*)l, 16, 0, 0);
}
DI float xhalf_max(float m) {
    auto r = __builtin_amdgcn_permlane32_swap(__float_as_uint(m), __float_as_uint(m), false, false);
    return fmaxf(__uint_as_float(r[0]), __uint_as_float(r[1]));
}
#define STAMP(i) do {} while (0)
#define CSTAMP(i) do {} while (0)
#define STAMP_FLUSH() do {} while (0)

__global__ __launch_bounds__(512, 2) void swa_block(const u16* __restrict__ x, const u16* __restrict__ wqkv,
                                                    const u16* __restrict__ wo, u16* __restrict__ out, char* ws) {
    extern __shared__ __attribute__((aligned(16))) char smem_[];
    LAS char* lds = (LAS char*)smem_;
    const int tid = threadIdx.x;
    const int wave = __builtin_amdgcn_readfirstlane(tid >> 6);
    const int lane = tid & 63, r32 = lane & 31, hh = lane >> 5;
    const int bid = blockIdx.x, g = bid & 7, ii = bid >> 3;
    const int hp = (g & 1) * 8 + (ii & 7);
    const int ck = (g >> 1) * 4 + (ii >> 3);
    const __amdgpu_buffer_rsrc_t wsr = __builtin_amdgcn_make_buffer_rsrc(ws, 0, WS_END, 0x00020000);
    const unsigned kvbase = WS_KV + (unsigned)(hp * 16 + ck) * 16384u;
    unsigned stampv = 0; (void)stampv;
    STAMP(0); CSTAMP(62);

    if (wave >= 4) {
        const int lw = wave - 4;
        const u16* xs[2];
        {
            const int c0 = (lane & 7) ^ (lane >> 4);
            const u16* xb = x + (size_t)(ck * 128 + lw * 32 + (lane >> 3)) * 512;
            xs[0] = xb + c0 * 8;
            xs[1] = xb + (c0 ^ 4) * 8;
        }
        const u16* wsrc = wqkv + (size_t)(lw * 16 + (lane >> 2)) * 1536 + hp * 32 + (lane & 3) * 8;
        auto issue = [&](int st, int slot) {
            LAS char* sb = lds + slot * SLOT;
#pragma unroll
            for (int i = 0; i < 4; ++i) glds16(xs[i & 1] + i * 8 * 512 + st * 64, sb + (lw * 32 + i * 8) * 128);
            const u16* wp = wsrc + (size_t)st * 64 * 1536;
            glds16(wp + 512, sb + 16384 + lw * 1024);
            glds16(wp + 1024, sb + 16384 + 4096 + lw * 1024);
            glds16(wp, sb + 16384 + 8192 + lw * 1024);
        };
        issue(0, 0); issue(1, 1); issue(2, 2); issue(3, 3); issue(4, 4);
        if (lw == 0) { u32x4 one = {0x3F803F80u, 0x3F803F80u, 0x3F803F80u, 0x3F803F80u}; *(LAS u32x4*)(lds + L_ONES + lane * 16) = one; }
        STAMP(16);
        WAITV(28); STAMP(20); bar();
        WAITV(21); STAMP(21); bar(); issue(5, 0);
        WAITV(21); STAMP(22); bar(); issue(6, 1);
        WAITV(21); STAMP(23); bar(); issue(7, 2);
        WAITV(21); STAMP(24); bar();
        WAITV(14); STAMP(25); bar();
        WAITV(7); STAMP(26); bar();
        WAITV(0); STAMP(27); bar();
    } else {
        unsigned epoch = __builtin_amdgcn_raw_buffer_load_b32(wsr, WS_EPOCH, 0, 16);
        {
            const u32x4 sv = {SENT, SENT, SENT, SENT};
#pragma unroll
            for (int i = 0; i < 4; ++i) __builtin_amdgcn_raw_buffer_store_b128(sv, wsr, kvbase + (wave * 4 + i) * 1024 + lane * 16, 0, 16);
#pragma unroll
            for (int i = 0; i < 2; ++i)
                __builtin_amdgcn_raw_buffer_store_b128(sv, wsr, WS_O + (unsigned)((ck * 4 + wave) * 32 + hp * 2 + i) * 1024 + lane * 16, 0, 16);
        }
        const unsigned tag = __builtin_amdgcn_readfirstlane(epoch) + 1u;
        int xoff[4];
        {
            const int u = hh ^ ((r32 >> 1) & 7);
#pragma unroll
            for (int ks = 0; ks < 4; ++ks) xoff[ks] = (wave * 32 + r32) * 128 + (((2 * ks) ^ u) << 4);
        }
        const int woff = 16384 + (8 * hh + ((lane & 15) >> 2)) * 64 + ((lane >> 4) & 1) * 32 + (lane & 3) * 8;
        f32x16 aK = {}, aV = {}, aQ = {};
        STAMP(17);
        WAITV(0);
        STAMP(18);
        u32x4 sflags = {0, 0, 0, 0};
        for (int st = 0; st < 8; ++st) {
            bar();
            STAMP(30 + st);
            if (st == 0) { STAMP(1); if (tid == 0) __builtin_amdgcn_raw_buffer_store_b32(tag, wsr, WS_START + bid * 4, 0, 16); }
            if (st == 6 && wave == 1) sflags = __builtin_amdgcn_raw_buffer_load_b128(wsr, WS_START + lane * 16, 0, 16);
            LAS char* sb = lds + (st >= 5 ? st - 5 : st) * SLOT;
#pragma unroll
            for (int ks = 0; ks < 4; ++ks) {
                bf16x8 xf = *(LAS bf16x8*)(sb + xoff[ks]);
                LAS char* wp = sb + woff + ks * 1024;
                bf16x8 wk = tr2(wp, wp + 256);
                bf16x8 wv = tr2(wp + 4096, wp + 4096 + 256);
                bf16x8 wq = tr2(wp + 8192, wp + 8192 + 256);
                aK = MFMA32(wk, xf, aK);
                aV = MFMA32(xf, wv, aV);
                aQ = MFMA32(wq, xf, aQ);
            }
        }
        STAMP(2);
        const u32x4 k0 = sanitize(pack8(aK, 0)), k1 = sanitize(pack8(aK, 8));
        const u32x4 v0 = sanitize(pack8(aV, 0)), v1 = sanitize(pack8(aV, 8));
        const int vo = (r32 >> 4) * 4096 + wave * 1024 + hh * 256 + (r32 & 15) * 16;
        __builtin_amdgcn_raw_buffer_store_b128(k0, wsr, kvbase + wave * 1024 + lane * 16, 0, 16);
        __builtin_amdgcn_raw_buffer_store_b128(k1, wsr, kvbase + 4096 + wave * 1024 + lane * 16, 0, 16);
        __builtin_amdgcn_raw_buffer_store_b128(v0, wsr, kvbase + 8192 + vo, 0, 16);
        __builtin_amdgcn_raw_buffer_store_b128(v1, wsr, kvbase + 8192 + vo + 512, 0, 16);
        *(LAS u32x4*)(lds + L_K + (8 + wave) * 1024 + lane * 16) = k0;
        *(LAS u32x4*)(lds + L_K + 12288 + (8 + wave) * 1024 + lane * 16) = k1;
        const int vl = (r32 >> 4) * 12288 + (8 + wave) * 1024 + hh * 256 + (r32 & 15) * 16;
        *(LAS u32x4*)(lds + L_V + vl) = v0;
        *(LAS u32x4*)(lds + L_V + vl + 512) = v1;
        *(LAS u32x4*)(lds + L_Q + wave * 1024 + lane * 16) = pack8(aQ, 0);
        *(LAS u32x4*)(lds + L_Q + 4096 + wave * 1024 + lane * 16) = pack8(aQ, 8);
        if (wave == 1) {
            for (int spin = 0; spin < SPIN_CAP; ++spin) {
                const bool ok = sflags.x == tag && sflags.y == tag && sflags.z == tag && sflags.w == tag;
                if (__all(ok)) break;
                __builtin_amdgcn_s_sleep(1);
                asm volatile("" ::: "memory");
                sflags = __builtin_amdgcn_raw_buffer_load_b128(wsr, WS_START + lane * 16, 0, 16);
            }
            if (bid == 0 && lane == 0) __builtin_amdgcn_raw_buffer_store_b32(tag, wsr, WS_EPOCH, 0, 16);
        }
    }
    bar();
    STAMP(3);

    const int hd = wave & 1;
    const int qb = (wave < 4) ? 3 - (wave >> 1) : ((wave - 4) >> 1);
    const int orow = g * 256 + (ii >> 3) * 64, ocol = (ii & 7) * 64;
    auto issue_wo = [&]() {
        const int krow = lane >> 3, s16 = lane & 7;
        const int src16 = (((s16 >> 1) ^ ((krow >> 1) & 3)) << 1) | (s16 & 1);
        const u16* p = wo + (size_t)(wave * 64 + krow) * 512 + ocol + src16 * 8;
#pragma unroll
        for (int i = 0; i < 8; ++i) glds16(p + (size_t)i * 8 * 512, lds + L_WO + (wave * 64 + i * 8) * 128);
    };
    if (wave == 4 || wave == 5) {
#pragma unroll
        for (int cc = 0; cc < 2; ++cc) {
            const int c2 = ck - 1 - cc;
            if (c2 >= 0) {
                const unsigned src = WS_KV + (unsigned)(hp * 16 + c2) * 16384u + hd * 4096 + lane * 16;
                u32x4 d[8];
                for (int spin = 0; spin < SPIN_CAP; ++spin) {
                    asm volatile("" ::: "memory");
#pragma unroll
                    for (int i = 0; i < 8; ++i) d[i] = __builtin_amdgcn_raw_buffer_load_b128(wsr, src + (i >> 2) * 8192 + (i & 3) * 1024, 0, 16);
                    unsigned mx = 0;
#pragma unroll
                    for (int i = 0; i < 8; ++i) mx = max(max(mx, max(d[i].x, d[i].y)), max(d[i].z, d[i].w));
                    if (__all(mx != SENT)) break;
                }
#pragma unroll
                for (int i = 0; i < 8; ++i)
                    *(LAS u32x4*)(lds + ((i >> 2) ? L_V : L_K) + hd * 12288 + ((1 - cc) * 4 + (i & 3)) * 1024 + lane * 16) = d[i];
            }
        }
        STAMP(4);
    }
    issue_wo();

    const bf16x8 qf = *(LAS bf16x8*)(lds + L_Q + hd * 4096 + qb * 1024 + lane * 16);
    LAS char* kp = lds + L_K + hd * 12288 + qb * 1024 + lane * 16;
    const int va0 = (r32 < 16) ? (L_V + hd * 12288 + qb * 1024 + hh * 256 + r32 * 16) : (L_ONES + (r32 - 16) * 16);
    const int vstr = (r32 < 16) ? 1024 : 0;
    const int tmin = (ck >= 2) ? 1 : max(1, 8 - 4 * ck - qb);
    const bool has_first = ck >= 2;
    f32x16 O = {};
    float mrun = -__builtin_inff();
    for (int grp = 0; grp < 2; ++grp) {
        const int lo = grp ? tmin : 8 - qb, hi = grp ? 8 - qb : 8;
        if (grp) { bar(); STAMP(5); }
        f32x16 S[9];
        bf16x8 kf[9];
#pragma unroll
        for (int t = 0; t < 9; ++t) kf[t] = *(LAS bf16x8*)(kp + t * 1024);
        const f32x16 zero = {};
#pragma unroll
        for (int t = 1; t < 8; ++t) if (t >= lo && t < hi) S[t] = MFMA32(kf[t], qf, zero);
        if (grp) {
            S[8] = MFMA32(kf[8], qf, zero);
            if (has_first) S[0] = MFMA32(kf[0], qf, zero);
            else {
#pragma unroll
                for (int i = 0; i < 16; ++i) S[0][i] = -__builtin_inff();
            }
#pragma unroll
            for (int i = 0; i < 16; ++i) {
                const int key = (i & 3) + 8 * (i >> 2) + 4 * hh;
                S[0][i] = (key > r32) ? S[0][i] : S[8][i];
            }
        }
        float m = mrun;
#pragma unroll
        for (int t = 0; t < 8; ++t) if (t ? (t >= lo && t < hi) : (grp != 0)) {
#pragma unroll
            for (int i = 0; i < 16; i += 2) m = fmaxf(fmaxf(m, S[t][i]), S[t][i + 1]);
        }
        m = round_bf16(xhalf_max(m));
        if (grp) {
            const float alpha = __builtin_amdgcn_exp2f((mrun - m) * CEXP);
#pragma unroll
            for (int i = 0; i < 9; ++i) O[i] *= alpha;
        }
        mrun = m;
        const float nmc = -m * CEXP;
#pragma unroll
        for (int t = 1; t < 8; ++t) if (t >= lo && t < hi) {
            LAS char* vp = lds + va0 + t * vstr;
            const bf16x8 vf0 = *(LAS bf16x8*)(vp), vf1 = *(LAS bf16x8*)(vp + 512);
            f32x16 e;
#pragma unroll
            for (int i = 0; i < 16; ++i) e[i] = __builtin_amdgcn_exp2f(__builtin_fmaf(round_bf16(S[t][i]), CEXP, nmc));
            O = MFMA32(vf0, as_bf(pack8(e, 0)), O);
            O = MFMA32(vf1, as_bf(pack8(e, 8)), O);
        }
        if (grp) {
            f32x16 e;
#pragma unroll
            for (int i = 0; i < 16; ++i) e[i] = __builtin_amdgcn_exp2f(__builtin_fmaf(round_bf16(S[0][i]), CEXP, nmc));
            const u32x4 p0 = pack8(e, 0), p1 = pack8(e, 8);
            u32x4 mk0, mk1;
#pragma unroll
            for (int j = 0; j < 4; ++j) {
                const int ka = ((2 * j) & 3) + 8 * ((2 * j) >> 2) + 4 * hh;
                mk0[j] = ((ka > r32) ? 0xFFFFu : 0u) | ((ka + 1 > r32) ? 0xFFFF0000u : 0u);
                mk1[j] = ((ka + 16 > r32) ? 0xFFFFu : 0u) | ((ka + 17 > r32) ? 0xFFFF0000u : 0u);
            }
            {
                LAS char* vp = lds + va0 + 8 * vstr;
                const bf16x8 vf0 = *(LAS bf16x8*)(vp), vf1 = *(LAS bf16x8*)(vp + 512);
                O = MFMA32(vf0, as_bf(p0 & ~mk0), O);
                O = MFMA32(vf1, as_bf(p1 & ~mk1), O);
            }
            if (has_first) {
                LAS char* vp = lds + va0;
                const bf16x8 vf0 = *(LAS bf16x8*)(vp), vf1 = *(LAS bf16x8*)(vp + 512);
                O = MFMA32(vf0, as_bf(p0 & mk0), O);
                O = MFMA32(vf1, as_bf(p1 & mk1), O);
            }
        }
    }
    {
        const float rl = 1.0f / O[8];
        f32x16 on;
#pragma unroll
        for (int i = 0; i < 8; ++i) on[i] = O[i] * rl;
        const u32x4 ov = sanitize(pack8(on, 0));
        __builtin_amdgcn_raw_buffer_store_b128(ov, wsr, WS_O + (unsigned)((ck * 4 + qb) * 32 + hp * 2 + hd) * 1024 + lane * 16, 0, 16);
    }
    STAMP(6);
    WAITV(1);
    bar();
    STAMP(7);

    {
        const int ct = wave & 3, th = wave >> 2;
        const unsigned src = WS_O + (unsigned)((orow >> 5) * 32 + wave * 8) * 1024 + lane * 16;
        u32x4 d[8];
#pragma unroll
        for (int i = 0; i < 8; ++i) d[i] = __builtin_amdgcn_raw_buffer_load_b128(wsr, src + i * 1024, 0, 16);
        bf16x8 wf[16];
        {
            const int gq = lane >> 4, q = (lane & 15) >> 2, p = lane & 3;
            const int kr = 16 * (gq >> 1) + 4 * (gq & 1) + q;
            const int a0 = L_WO + kr * 128 + ((ct ^ ((kr >> 1) & 3)) << 5) + p * 8;
#pragma unroll
            for (int s = 0; s < 16; ++s) wf[s] = tr2(lds + a0 + s * 4096, lds + a0 + s * 4096 + 1024);
        }
        for (int spin = 0; spin < SPIN_CAP; ++spin) {
            unsigned mx = 0;
#pragma unroll
            for (int i = 0; i < 8; ++i) mx = max(max(mx, max(d[i].x, d[i].y)), max(d[i].z, d[i].w));
            if (__all(mx != SENT)) break;
            asm volatile("" ::: "memory");
#pragma unroll
            for (int i = 0; i < 8; ++i) d[i] = __builtin_amdgcn_raw_buffer_load_b128(wsr, src + i * 1024, 0, 16);
        }
        STAMP(8);
#pragma unroll
        for (int i = 0; i < 8; ++i) *(LAS u32x4*)(lds + L_OST + (wave * 8 + i) * 1024 + lane * 16) = d[i];
        bar();
        STAMP(9);
        f32x4 c0 = {}, c1 = {};
        const int gq = lane >> 4;
        LAS char* op = lds + L_OST + th * 32768 + (gq >> 1) * 1024 + (gq & 1) * 512 + (lane & 15) * 16;
#pragma unroll
        for (int s = 0; s < 16; ++s) {
            const bf16x8 o0 = *(LAS bf16x8*)(op + s * 2048), o1 = *(LAS bf16x8*)(op + s * 2048 + 256);
            c0 = MFMA16(wf[s], o0, c0);
            c1 = MFMA16(wf[s], o1, c1);
        }
        u16* dst = out + (size_t)(orow + th * 32 + (lane & 15)) * 512 + ocol + ct * 16 + gq * 4;
        u32x2 r0 = {pack2(c0[0], c0[1]), pack2(c0[2], c0[3])}, r1 = {pack2(c1[0], c1[1]), pack2(c1[2], c1[3])};
        *(u32x2*)dst = r0;
        *(u32x2*)(dst + 16 * 512) = r1;
        STAMP(10); CSTAMP(63); STAMP_FLUSH();
    }
}

extern "C" void kernel_launch(void* const* d_in, const int* in_sizes, int n_in, void* d_out, int out_size, void* d_ws,
                              size_t ws_size, hipStream_t stream) {
    (void)in_sizes; (void)n_in; (void)out_size; (void)ws_size;
    (void)hipFuncSetAttribute((const void*)swa_block, hipFuncAttributeMaxDynamicSharedMemorySize, LDS_BYTES);
    swa_block<<<256, 512, LDS_BYTES, stream>>>((const u16*)d_in[0], (const u16*)d_in[1], (const u16*)d_in[2], (u16*)d_out, (char*)d_ws);
}
```

```cpp
#define AMPJ 10
#define AMPM 0
#define TIMELINE 1
#include <hip/hip_runtime.h>
#include <stdint.h>

typedef short bf16x8 __attribute__((ext_vector_type(8)));
typedef short s16x4 __attribute__((ext_vector_type(4)));
typedef float f32x16 __attribute__((ext_vector_type(16)));
typedef float f32x4 __attribute__((ext_vector_type(4)));
typedef float f32x2 __attribute__((ext_vector_type(2)));
typedef unsigned u32x4 __attribute__((ext_vector_type(4)));
typedef unsigned u32x2 __attribute__((ext_vector_type(2)));
typedef __bf16 bf16x2 __attribute__((ext_vector_type(2)));
typedef unsigned short u16;
#define LAS __attribute__((address_space(3)))
#define GAS __attribute__((address_space(1)))
#define DI __device__ __forceinline__

constexpr unsigned WS_EPOCH = 0;
constexpr unsigned WS_START = 1024;
constexpr unsigned WS_KV = 4096;
constexpr unsigned WS_O = WS_KV + 16 * 16 * 16384;
constexpr unsigned WS_END = WS_O + 64 * 32 * 1024;
constexpr unsigned WS_DBG = WS_END;
constexpr int SLOT = 28672;
constexpr int L_K = 3 * SLOT;
constexpr int L_V = L_K + 24576;
constexpr int L_Q = L_V + 24576;
constexpr int L_ONES = 5 * SLOT;
constexpr int L_WO = 0;
constexpr int L_OST = 65536;
constexpr int LDS_BYTES = L_ONES + 1024;

constexpr float CEXP = 0.36067376022224085f;
constexpr unsigned SENT = 0xFFFFFFFFu;
constexpr int SPIN_CAP = 1 << 18;

DI float round_bf16(float s) {
    f32x2 v = {0.0f, s};
    return __builtin_bit_cast(float, __builtin_convertvector(v, bf16x2));
}
DI unsigned pack2(float a, float b) {
    f32x2 v = {a, b};
    return __builtin_bit_cast(unsigned, __builtin_convertvector(v, bf16x2));
}
DI u32x4 pack8(const f32x16& a, int o) {
    u32x4 r = {pack2(a[o], a[o + 1]), pack2(a[o + 2], a[o + 3]), pack2(a[o + 4], a[o + 5]), pack2(a[o + 6], a[o + 7])};
    return r;
}
DI u32x4 sanitize(u32x4 v) {
    u32x4 r = {min(v.x, SENT - 1), min(v.y, SENT - 1), min(v.z, SENT - 1), min(v.w, SENT - 1)};
    return r;
}
DI bf16x8 as_bf(u32x4 v) { return __builtin_bit_cast(bf16x8, v); }
DI void bar() { asm volatile("s_waitcnt lgkmcnt(0)" ::: "memory"); __builtin_amdgcn_s_barrier(); asm volatile("" ::: "memory"); }
#define WAITV(n) asm volatile("s_waitcnt vmcnt(" #n ")" ::: "memory")
#define MFMA32(a, b, c) __builtin_amdgcn_mfma_f32_32x32x16_bf16((a), (b), (c), 0, 0, 0)
#define MFMA16(a, b, c) __builtin_amdgcn_mfma_f32_16x16x32_bf16((a), (b), (c), 0, 0, 0)
DI s16x4 trread(LAS char* p) { return __builtin_amdgcn_ds_read_tr16_b64_v4i16((LAS s16x4*)p); }
DI bf16x8 tr2(LAS char* p0, LAS char* p1) {
    s16x4 a = trread(p0), b = trread(p1);
    return __builtin_shufflevector(a, b, 0, 1, 2, 3, 4, 5, 6, 7);
}
DI void glds16(const void* g, LAS char* l) {
    __builtin_amdgcn_global_load_lds((const GAS unsigned*)g, (LAS unsigned*)l, 16, 0, 0);
}
DI float xhalf_max(float m) {
    auto r = __builtin_amdgcn_permlane32_swap(__float_as_uint(m), __float_as_uint(m), false, false);
    return fmaxf(__uint_as_float(r[0]), __uint_as_float(r[1]));
}
#define STAMP(i) do { unsigned long long t_ = __builtin_amdgcn_s_memrealtime(); stampv = (lane == (i)) ? (unsigned)t_ : stampv; } while (0)
#define CSTAMP(i) do { unsigned long long t_ = __builtin_amdgcn_s_memtime(); stampv = (lane == (i)) ? (unsigned)t_ : stampv; } while (0)
#define STAMP_FLUSH() do { *(volatile unsigned*)(ws + WS_DBG + ((size_t)(blockIdx.x * 8 + wave) * 64 + lane) * 4) = stampv; } while (0)

__global__ __launch_bounds__(512, 2) void swa_block(const u16* __restrict__ x, const u16* __restrict__ wqkv,
                                                    const u16* __restrict__ wo, u16* __restrict__ out, char* ws) {
    extern __shared__ __attribute__((aligned(16))) char smem_[];
    LAS char* lds = (LAS char*)smem_;
    const int tid = threadIdx.x;
    const int wave = __builtin_amdgcn_readfirstlane(tid >> 6);
    const int lane = tid & 63, r32 = lane & 31, hh = lane >> 5;
    const int bid = blockIdx.x, g = bid & 7, ii = bid >> 3;
    const int hp = (g & 1) * 8 + (ii & 7);
    const int ck = (g >> 1) * 4 + (ii >> 3);
    const __amdgpu_buffer_rsrc_t wsr = __builtin_amdgcn_make_buffer_rsrc(ws, 0, WS_END, 0x00020000);
    const unsigned kvbase = WS_KV + (unsigned)(hp * 16 + ck) * 16384u;
    unsigned stampv = 0; (void)stampv;
    STAMP(0); CSTAMP(62);

    if (wave >= 4) {
        const int lw = wave - 4;
        const u16* xs[2];
        {
            const int c0 = (lane & 7) ^ (lane >> 4);
            const u16* xb = x + (size_t)(ck * 128 + lw * 32 + (lane >> 3)) * 512;
            xs[0] = xb + c0 * 8;
            xs[1] = xb + (c0 ^ 4) * 8;
        }
        const u16* wsrc = wqkv + (size_t)(lw * 16 + (lane >> 2)) * 1536 + hp * 32 + (lane & 3) * 8;
        auto issue = [&](int st, int slot) {
            LAS char* sb = lds + slot * SLOT;
#pragma unroll
            for (int i = 0; i < 4; ++i) glds16(xs[i & 1] + i * 8 * 512 + st * 64, sb + (lw * 32 + i * 8) * 128);
            const u16* wp = wsrc + (size_t)st * 64 * 1536;
            glds16(wp + 512, sb + 16384 + lw * 1024);
            glds16(wp + 1024, sb + 16384 + 4096 + lw * 1024);
            glds16(wp, sb + 16384 + 8192 + lw * 1024);
        };
        issue(0, 0); issue(1, 1); issue(2, 2); issue(3, 3); issue(4, 4);
        if (lw == 0) { u32x4 one = {0x3F803F80u, 0x3F803F80u, 0x3F803F80u, 0x3F803F80u}; *(LAS u32x4*)(lds + L_ONES + lane * 16) = one; }
        STAMP(16);
        WAITV(28); STAMP(20); bar();
        WAITV(21); STAMP(21); bar(); issue(5, 0);
        WAITV(21); STAMP(22); bar(); issue(6, 1);
        WAITV(21); STAMP(23); bar(); issue(7, 2);
        WAITV(21); STAMP(24); bar();
        WAITV(14); STAMP(25); bar();
        WAITV(7); STAMP(26); bar();
        WAITV(0); STAMP(27); bar();
    } else {
        unsigned epoch = __builtin_amdgcn_raw_buffer_load_b32(wsr, WS_EPOCH, 0, 16);
        {
            const u32x4 sv = {SENT, SENT, SENT, SENT};
#pragma unroll
            for (int i = 0; i < 4; ++i) __builtin_amdgcn_raw_buffer_store_b128(sv, wsr, kvbase + (wave * 4 + i) * 1024 + lane * 16, 0, 16);
#pragma unroll
            for (int i = 0; i < 2; ++i)
                __builtin_amdgcn_raw_buffer_store_b128(sv, wsr, WS_O + (unsigned)((ck * 4 + wave) * 32 + hp * 2 + i) * 1024 + lane * 16, 0, 16);
        }
        const unsigned tag = __builtin_amdgcn_readfirstlane(epoch) + 1u;
        int xoff[4];
        {
            const int u = hh ^ ((r32 >> 1) & 7);
#pragma unroll
            for (int ks = 0; ks < 4; ++ks) xoff[ks] = (wave * 32 + r32) * 128 + (((2 * ks) ^ u) << 4);
        }
        const int woff = 16384 + (8 * hh + ((lane & 15) >> 2)) * 64 + ((lane >> 4) & 1) * 32 + (lane & 3) * 8;
        f32x16 aK = {}, aV = {}, aQ = {};
        STAMP(17);
        WAITV(0);
        STAMP(18);
        u32x4 sflags = {0, 0, 0, 0};
        for (int st = 0; st < 8; ++st) {
            bar();
            STAMP(30 + st);
            if (st == 0) { STAMP(1); if (tid == 0) __builtin_amdgcn_raw_buffer_store_b32(tag, wsr, WS_START + bid * 4, 0, 16); }
            if (st == 6 && wave == 1) sflags = __builtin_amdgcn_raw_buffer_load_b128(wsr, WS_START + lane * 16, 0, 16);
            LAS char* sb = lds + (st >= 5 ? st - 5 : st) * SLOT;
#pragma unroll
            for (int ks = 0; ks < 4; ++ks) {
                bf16x8 xf = *(LAS bf16x8*)(sb + xoff[ks]);
                LAS char* wp = sb + woff + ks * 1024;
                bf16x8 wk = tr2(wp, wp + 256);
                bf16x8 wv = tr2(wp + 4096, wp + 4096 + 256);
                bf16x8 wq = tr2(wp + 8192, wp + 8192 + 256);
                aK = MFMA32(wk, xf, aK);
                aV = MFMA32(xf, wv, aV);
                aQ = MFMA32(wq, xf, aQ);
            }
        }
        STAMP(2);
        const u32x4 k0 = sanitize(pack8(aK, 0)), k1 = sanitize(pack8(aK, 8));
        const u32x4 v0 = sanitize(pack8(aV, 0)), v1 = sanitize(pack8(aV, 8));
        const int vo = (r32 >> 4) * 4096 + wave * 1024 + hh * 256 + (r32 & 15) * 16;
        __builtin_amdgcn_raw_buffer_store_b128(k0, wsr, kvbase + wave * 1024 + lane * 16, 0, 16);
        __builtin_amdgcn_raw_buffer_store_b128(k1, wsr, kvbase + 4096 + wave * 1024 + lane * 16, 0, 16);
        __builtin_amdgcn_raw_buffer_store_b128(v0, wsr, kvbase + 8192 + vo, 0, 16);
        __builtin_amdgcn_raw_buffer_store_b128(v1, wsr, kvbase + 8192 + vo + 512, 0, 16);
        *(LAS u32x4*)(lds + L_K + (8 + wave) * 1024 + lane * 16) = k0;
        *(LAS u32x4*)(lds + L_K + 12288 + (8 + wave) * 1024 + lane * 16) = k1;
        const int vl = (r32 >> 4) * 12288 + (8 + wave) * 1024 + hh * 256 + (r32 & 15) * 16;
        *(LAS u32x4*)(lds + L_V + vl) = v0;
        *(LAS u32x4*)(lds + L_V + vl + 512) = v1;
        *(LAS u32x4*)(lds + L_Q + wave * 1024 + lane * 16) = pack8(aQ, 0);
        *(LAS u32x4*)(lds + L_Q + 4096 + wave * 1024 + lane * 16) = pack8(aQ, 8);
        if (wave == 1) {
            for (int spin = 0; spin < SPIN_CAP; ++spin) {
                const bool ok = sflags.x == tag && sflags.y == tag && sflags.z == tag && sflags.w == tag;
                if (__all(ok)) break;
                __builtin_amdgcn_s_sleep(1);
                asm volatile("" ::: "memory");
                sflags = __builtin_amdgcn_raw_buffer_load_b128(wsr, WS_START + lane * 16, 0, 16);
            }
            if (bid == 0 && lane == 0) __builtin_amdgcn_raw_buffer_store_b32(tag, wsr, WS_EPOCH, 0, 16);
        }
    }
    bar();
    STAMP(3);

    const int hd = wave & 1;
    const int qb = (wave < 4) ? 3 - (wave >> 1) : ((wave - 4) >> 1);
    const int orow = g * 256 + (ii >> 3) * 64, ocol = (ii & 7) * 64;
    auto issue_wo = [&]() {
        const int krow = lane >> 3, s16 = lane & 7;
        const int src16 = (((s16 >> 1) ^ ((krow >> 1) & 3)) << 1) | (s16 & 1);
        const u16* p = wo + (size_t)(wave * 64 + krow) * 512 + ocol + src16 * 8;
#pragma unroll
        for (int i = 0; i < 8; ++i) glds16(p + (size_t)i * 8 * 512, lds + L_WO + (wave * 64 + i * 8) * 128);
    };
    if (wave == 4 || wave == 5) {
#pragma unroll
        for (int cc = 0; cc < 2; ++cc) {
            const int c2 = ck - 1 - cc;
            if (c2 >= 0) {
                const unsigned src = WS_KV + (unsigned)(hp * 16 + c2) * 16384u + hd * 4096 + lane * 16;
                u32x4 d[8];
                for (int spin = 0; spin < SPIN_CAP; ++spin) {
                    asm volatile("" ::: "memory");
#pragma unroll
                    for (int i = 0; i < 8; ++i) d[i] = __builtin_amdgcn_raw_buffer_load_b128(wsr, src + (i >> 2) * 8192 + (i & 3) * 1024, 0, 16);
                    unsigned mx = 0;
#pragma unroll
                    for (int i = 0; i < 8; ++i) mx = max(max(mx, max(d[i].x, d[i].y)), max(d[i].z, d[i].w));
                    if (__all(mx != SENT)) break;
                }
#pragma unroll
                for (int i = 0; i < 8; ++i)
                    *(LAS u32x4*)(lds + ((i >> 2) ? L_V : L_K) + hd * 12288 + ((1 - cc) * 4 + (i & 3)) * 1024 + lane * 16) = d[i];
            }
        }
        STAMP(4);
    }
    issue_wo();

    const bf16x8 qf = *(LAS bf16x8*)(lds + L_Q + hd * 4096 + qb * 1024 + lane * 16);
    LAS char* kp = lds + L_K + hd * 12288 + qb * 1024 + lane * 16;
    const int va0 = (r32 < 16) ? (L_V + hd * 12288 + qb * 1024 + hh * 256 + r32 * 16) : (L_ONES + (r32 - 16) * 16);
    const int vstr = (r32 < 16) ? 1024 : 0;
    const int tmin = (ck >= 2) ? 1 : max(1, 8 - 4 * ck - qb);
    const bool has_first = ck >= 2;
    f32x16 O = {};
    float mrun = -__builtin_inff();
    for (int grp = 0; grp < 2; ++grp) {
        const int lo = grp ? tmin : 8 - qb, hi = grp ? 8 - qb : 8;
        if (grp) { bar(); STAMP(5); }
        f32x16 S[9];
        bf16x8 kf[9];
#pragma unroll
        for (int t = 0; t < 9; ++t) kf[t] = *(LAS bf16x8*)(kp + t * 1024);
        const f32x16 zero = {};
#pragma unroll
        for (int t = 1; t < 8; ++t) if (t >= lo && t < hi) S[t] = MFMA32(kf[t], qf, zero);
        if (grp) {
            S[8] = MFMA32(kf[8], qf, zero);
            if (has_first) S[0] = MFMA32(kf[0], qf, zero);
            else {
#pragma unroll
                for (int i = 0; i < 16; ++i) S[0][i] = -__builtin_inff();
            }
#pragma unroll
            for (int i = 0; i < 16; ++i) {
                const int key = (i & 3) + 8 * (i >> 2) + 4 * hh;
                S[0][i] = (key > r32) ? S[0][i] : S[8][i];
            }
        }
        float m = mrun;
#pragma unroll
        for (int t = 0; t < 8; ++t) if (t ? (t >= lo && t < hi) : (grp != 0)) {
#pragma unroll
            for (int i = 0; i < 16; i += 2) m = fmaxf(fmaxf(m, S[t][i]), S[t][i + 1]);
        }
        m = round_bf16(xhalf_max(m));
        if (grp) {
            const float alpha = __builtin_amdgcn_exp2f((mrun - m) * CEXP);
#pragma unroll
            for (int i = 0; i < 9; ++i) O[i] *= alpha;
        }
        mrun = m;
        const float nmc = -m * CEXP;
#pragma unroll
        for (int t = 1; t < 8; ++t) if (t >= lo && t < hi) {
            LAS char* vp = lds + va0 + t * vstr;
            const bf16x8 vf0 = *(LAS bf16x8*)(vp), vf1 = *(LAS bf16x8*)(vp + 512);
            f32x16 e;
#pragma unroll
            for (int i = 0; i < 16; ++i) e[i] = __builtin_amdgcn_exp2f(__builtin_fmaf(round_bf16(S[t][i]), CEXP, nmc));
            O = MFMA32(vf0, as_bf(pack8(e, 0)), O);
            O = MFMA32(vf1, as_bf(pack8(e, 8)), O);
        }
        if (grp) {
            f32x16 e;
#pragma unroll
            for (int i = 0; i < 16; ++i) e[i] = __builtin_amdgcn_exp2f(__builtin_fmaf(round_bf16(S[0][i]), CEXP, nmc));
            const u32x4 p0 = pack8(e, 0), p1 = pack8(e, 8);
            u32x4 mk0, mk1;
#pragma unroll
            for (int j = 0; j < 4; ++j) {
                const int ka = ((2 * j) & 3) + 8 * ((2 * j) >> 2) + 4 * hh;
                mk0[j] = ((ka > r32) ? 0xFFFFu : 0u) | ((ka + 1 > r32) ? 0xFFFF0000u : 0u);
                mk1[j] = ((ka + 16 > r32) ? 0xFFFFu : 0u) | ((ka + 17 > r32) ? 0xFFFF0000u : 0u);
            }
            {
                LAS char* vp = lds + va0 + 8 * vstr;
                const bf16x8 vf0 = *(LAS bf16x8*)(vp), vf1 = *(LAS bf16x8*)(vp + 512);
                O = MFMA32(vf0, as_bf(p0 & ~mk0), O);
                O = MFMA32(vf1, as_bf(p1 & ~mk1), O);
            }
            if (has_first) {
                LAS char* vp = lds + va0;
                const bf16x8 vf0 = *(LAS bf16x8*)(vp), vf1 = *(LAS bf16x8*)(vp + 512);
                O = MFMA32(vf0, as_bf(p0 & mk0), O);
                O = MFMA32(vf1, as_bf(p1 & mk1), O);
            }
        }
    }
    {
        const float rl = 1.0f / O[8];
        f32x16 on;
#pragma unroll
        for (int i = 0; i < 8; ++i) on[i] = O[i] * rl;
        const u32x4 ov = sanitize(pack8(on, 0));
        __builtin_amdgcn_raw_buffer_store_b128(ov, wsr, WS_O + (unsigned)((ck * 4 + qb) * 32 + hp * 2 + hd) * 1024 + lane * 16, 0, 16);
    }
    STAMP(6);
    WAITV(1);
    bar();
    STAMP(7);

    {
        const int ct = wave & 3, th = wave >> 2;
        const unsigned src = WS_O + (unsigned)((orow >> 5) * 32 + wave * 8) * 1024 + lane * 16;
        u32x4 d[8];
#pragma unroll
        for (int i = 0; i < 8; ++i) d[i] = __builtin_amdgcn_raw_buffer_load_b128(wsr, src + i * 1024, 0, 16);
        bf16x8 wf[16];
        {
            const int gq = lane >> 4, q = (lane & 15) >> 2, p = lane & 3;
            const int kr = 16 * (gq >> 1) + 4 * (gq & 1) + q;
            const int a0 = L_WO + kr * 128 + ((ct ^ ((kr >> 1) & 3)) << 5) + p * 8;
#pragma unroll
            for (int s = 0; s < 16; ++s) wf[s] = tr2(lds + a0 + s * 4096, lds + a0 + s * 4096 + 1024);
        }
        for (int spin = 0; spin < SPIN_CAP; ++spin) {
            unsigned mx = 0;
#pragma unroll
            for (int i = 0; i < 8; ++i) mx = max(max(mx, max(d[i].x, d[i].y)), max(d[i].z, d[i].w));
            if (__all(mx != SENT)) break;
            asm volatile("" ::: "memory");
#pragma unroll
            for (int i = 0; i < 8; ++i) d[i] = __builtin_amdgcn_raw_buffer_load_b128(wsr, src + i * 1024, 0, 16);
        }
        STAMP(8);
#pragma unroll
        for (int i = 0; i < 8; ++i) *(LAS u32x4*)(lds + L_OST + (wave * 8 + i) * 1024 + lane * 16) = d[i];
        bar();
        STAMP(9);
        f32x4 c0 = {}, c1 = {};
        const int gq = lane >> 4;
        LAS char* op = lds + L_OST + th * 32768 + (gq >> 1) * 1024 + (gq & 1) * 512 + (lane & 15) * 16;
#pragma unroll
        for (int s = 0; s < 16; ++s) {
            const bf16x8 o0 = *(LAS bf16x8*)(op + s * 2048), o1 = *(LAS bf16x8*)(op + s * 2048 + 256);
            c0 = MFMA16(wf[s], o0, c0);
            c1 = MFMA16(wf[s], o1, c1);
        }
        u16* dst = out + (size_t)(orow + th * 32 + (lane & 15)) * 512 + ocol + ct * 16 + gq * 4;
        u32x2 r0 = {pack2(c0[0], c0[1]), pack2(c0[2], c0[3])}, r1 = {pack2(c1[0], c1[1]), pack2(c1[2], c1[3])};
        *(u32x2*)dst = r0;
        *(u32x2*)(dst + 16 * 512) = r1;
        STAMP(10); CSTAMP(63); STAMP_FLUSH();
    }
}

__global__ void amp_kernel(char* ws, int j, int mult) {
    const unsigned* h = (const unsigned*)(ws + WS_DBG);
    const int lane = threadIdx.x;
    unsigned ref = h[0];
    int mn = 0x7fffffff, mx = -0x7fffffff;
    for (int i = lane; i < 2048; i += 64) {
        int a = (int)(h[i * 64] - ref); mn = min(mn, a);
        unsigned t = h[i * 64 + j]; if (t != 0) { int b = (int)(t - ref); mx = max(mx, b); }
    }
    for (int o = 32; o; o >>= 1) { mn = min(mn, __shfl_xor(mn, o)); mx = max(mx, __shfl_xor(mx, o)); }
    int X = mx - mn; if (X < 0) X = 0; if (X > 10000) X = 10000;
    unsigned long long t0 = __builtin_amdgcn_s_memrealtime();
    while (__builtin_amdgcn_s_memrealtime() - t0 < (unsigned long long)X * mult) __builtin_amdgcn_s_sleep(1);
}

extern "C" void kernel_launch(void* const* d_in, const int* in_sizes, int n_in, void* d_out, int out_size, void* d_ws,
                              size_t ws_size, hipStream_t stream) {
    (void)in_sizes; (void)n_in; (void)out_size; (void)ws_size;
    (void)hipFuncSetAttribute((const void*)swa_block, hipFuncAttributeMaxDynamicSharedMemorySize, LDS_BYTES);
    swa_block<<<256, 512, LDS_BYTES, stream>>>((const u16*)d_in[0], (const u16*)d_in[1], (const u16*)d_in[2], (u16*)d_out, (char*)d_ws);
    amp_kernel<<<1, 64, 0, stream>>>((char*)d_ws, AMPJ, AMPM);
}
```

```cpp
#include <hip/hip_runtime.h>
#include <stdint.h>

typedef short bf16x8 __attribute__((ext_vector_type(8)));
typedef short s16x4 __attribute__((ext_vector_type(4)));
typedef float f32x16 __attribute__((ext_vector_type(16)));
typedef float f32x4 __attribute__((ext_vector_type(4)));
typedef float f32x2 __attribute__((ext_vector_type(2)));
typedef unsigned u32x4 __attribute__((ext_vector_type(4)));
typedef unsigned u32x2 __attribute__((ext_vector_type(2)));
typedef __bf16 bf16x2 __attribute__((ext_vector_type(2)));
typedef unsigned short u16;
#define LAS __attribute__((address_space(3)))
#define GAS __attribute__((address_space(1)))
#define DI __device__ __forceinline__

constexpr unsigned WS_EPOCH = 0;
constexpr unsigned WS_START = 1024;
constexpr unsigned WS_KV = 4096;
constexpr unsigned WS_O = WS_KV + 16 * 16 * 16384;
constexpr unsigned WS_END = WS_O + 64 * 32 * 1024;
constexpr int SLOT = 28672;
constexpr int L_K = 3 * SLOT;
constexpr int L_V = L_K + 24576;
constexpr int L_Q = L_V + 24576;
constexpr int L_ONES = 5 * SLOT;
constexpr int L_WO = 0;
constexpr int L_OST = 65536;
constexpr int LDS_BYTES = L_ONES + 1024;

constexpr float CEXP = 0.36067376022224085f;
constexpr unsigned SENT = 0xFFFFFFFFu;
constexpr int SPIN_CAP = 1 << 18;

DI float round_bf16(float s) {
    f32x2 v = {0.0f, s};
    return __builtin_bit_cast(float, __builtin_convertvector(v, bf16x2));
}
DI unsigned pack2(float a, float b) {
    f32x2 v = {a, b};
    return __builtin_bit_cast(unsigned, __builtin_convertvector(v, bf16x2));
}
DI u32x4 pack8(const f32x16& a, int o) {
    u32x4 r = {pack2(a[o], a[o + 1]), pack2(a[o + 2], a[o + 3]), pack2(a[o + 4], a[o + 5]), pack2(a[o + 6], a[o + 7])};
    return r;
}
DI u32x4 sanitize(u32x4 v) {
    u32x4 r = {min(v.x, SENT - 1), min(v.y, SENT - 1), min(v.z, SENT - 1), min(v.w, SENT - 1)};
    return r;
}
DI bf16x8 as_bf(u32x4 v) { return __builtin_bit_cast(bf16x8, v); }
DI void bar() { asm volatile("s_waitcnt lgkmcnt(0)" ::: "memory"); __builtin_amdgcn_s_barrier(); asm volatile("" ::: "memory"); }
#define WAITV(n) asm volatile("s_waitcnt vmcnt(" #n ")" ::: "memory")
#define MFMA32(a, b, c) __builtin_amdgcn_mfma_f32_32x32x16_bf16((a), (b), (c), 0, 0, 0)
#define MFMA16(a, b, c) __builtin_amdgcn_mfma_f32_16x16x32_bf16((a), (b), (c), 0, 0, 0)
DI s16x4 trread(LAS char* p) { return __builtin_amdgcn_ds_read_tr16_b64_v4i16((LAS s16x4*)p); }
DI bf16x8 tr2(LAS char* p0, LAS char* p1) {
    s16x4 a = trread(p0), b = trread(p1);
    return __builtin_shufflevector(a, b, 0, 1, 2, 3, 4, 5, 6, 7);
}
DI void glds16(const void* g, LAS char* l) {
    __builtin_amdgcn_global_load_lds((const GAS unsigned*)g, (LAS unsigned*)l, 16, 0, 0);
}
DI float xhalf_max(float m) {
    auto r = __builtin_amdgcn_permlane32_swap(__float_as_uint(m), __float_as_uint(m), false, false);
    return fmaxf(__uint_as_float(r[0]), __uint_as_float(r[1]));
}
#define STAMP(i) do {} while (0)
#define CSTAMP(i) do {} while (0)
#define STAMP_FLUSH() do {} while (0)

__global__ __launch_bounds__(512, 2) void swa_block(const u16* __restrict__ x, const u16* __restrict__ wqkv,
                                                    const u16* __restrict__ wo, u16* __restrict__ out, char* ws) {
    extern __shared__ __attribute__((aligned(16))) char smem_[];
    LAS char* lds = (LAS char*)smem_;
    const int tid = threadIdx.x;
    const int wave = __builtin_amdgcn_readfirstlane(tid >> 6);
    const int lane = tid & 63, r32 = lane & 31, hh = lane >> 5;
    const int bid = blockIdx.x, g = bid & 7, ii = bid >> 3;
    const int hp = (g & 1) * 8 + (ii & 7);
    const int ck = (g >> 1) * 4 + (ii >> 3);
    const __amdgpu_buffer_rsrc_t wsr = __builtin_amdgcn_make_buffer_rsrc(ws, 0, WS_END, 0x00020000);
    const unsigned kvbase = WS_KV + (unsigned)(hp * 16 + ck) * 16384u;
    unsigned stampv = 0; (void)stampv;
    STAMP(0); CSTAMP(62);

    if (wave >= 4) {
        const int lw = wave - 4;
        const u16* xs[2];
        {
            const int c0 = (lane & 7) ^ (lane >> 4);
            const u16* xb = x + (size_t)(ck * 128 + lw * 32 + (lane >> 3)) * 512;
            xs[0] = xb + c0 * 8;
            xs[1] = xb + (c0 ^ 4) * 8;
        }
        const u16* wsrc = wqkv + (size_t)(lw * 16 + (lane >> 2)) * 1536 + hp * 32 + (lane & 3) * 8;
        auto issue = [&](int st, int slot) {
            LAS char* sb = lds + slot * SLOT;
#pragma unroll
            for (int i = 0; i < 4; ++i) glds16(xs[i & 1] + i * 8 * 512 + st * 64, sb + (lw * 32 + i * 8) * 128);
            const u16* wp = wsrc + (size_t)st * 64 * 1536;
            glds16(wp + 512, sb + 16384 + lw * 1024);
            glds16(wp + 1024, sb + 16384 + 4096 + lw * 1024);
            glds16(wp, sb + 16384 + 8192 + lw * 1024);
        };
        issue(0, 0); issue(1, 1); issue(2, 2); issue(3, 3); issue(4, 4);
        if (lw == 0) { u32x4 one = {0x3F803F80u, 0x3F803F80u, 0x3F803F80u, 0x3F803F80u}; *(LAS u32x4*)(lds + L_ONES + lane * 16) = one; }
        STAMP(16);
        WAITV(28); STAMP(20); bar();
        WAITV(21); STAMP(21); bar(); issue(5, 0);
        WAITV(21); STAMP(22); bar(); issue(6, 1);
        WAITV(21); STAMP(23); bar(); issue(7, 2);
        WAITV(21); STAMP(24); bar();
        WAITV(14); STAMP(25); bar();
        WAITV(7); STAMP(26); bar();
        WAITV(0); STAMP(27); bar();
    } else {
        unsigned epoch = __builtin_amdgcn_raw_buffer_load_b32(wsr, WS_EPOCH, 0, 16);
        {
            const u32x4 sv = {SENT, SENT, SENT, SENT};
#pragma unroll
            for (int i = 0; i < 4; ++i) __builtin_amdgcn_raw_buffer_store_b128(sv, wsr, kvbase + (wave * 4 + i) * 1024 + lane * 16, 0, 16);
#pragma unroll
            for (int i = 0; i < 2; ++i)
                __builtin_amdgcn_raw_buffer_store_b128(sv, wsr, WS_O + (unsigned)((ck * 4 + wave) * 32 + hp * 2 + i) * 1024 + lane * 16, 0, 16);
        }
        const unsigned tag = __builtin_amdgcn_readfirstlane(epoch) + 1u;
        int xoff[4];
        {
            const int u = hh ^ ((r32 >> 1) & 7);
#pragma unroll
            for (int ks = 0; ks < 4; ++ks) xoff[ks] = (wave * 32 + r32) * 128 + (((2 * ks) ^ u) << 4);
        }
        const int woff = 16384 + (8 * hh + ((lane & 15) >> 2)) * 64 + ((lane >> 4) & 1) * 32 + (lane & 3) * 8;
        f32x16 aK = {}, aV = {}, aQ = {};
        STAMP(17);
        WAITV(0);
        STAMP(18);
        u32x4 sflags = {0, 0, 0, 0};
        for (int st = 0; st < 8; ++st) {
            bar();
            STAMP(30 + st);
            if (st == 0) { STAMP(1); if (tid == 0) __builtin_amdgcn_raw_buffer_store_b32(tag, wsr, WS_START + bid * 4, 0, 16); }
            if (st == 6 && wave == 1) sflags = __builtin_amdgcn_raw_buffer_load_b128(wsr, WS_START + lane * 16, 0, 16);
            LAS char* sb = lds + (st >= 5 ? st - 5 : st) * SLOT;
#pragma unroll
            for (int ks = 0; ks < 4; ++ks) {
                bf16x8 xf = *(LAS bf16x8*)(sb + xoff[ks]);
                LAS char* wp = sb + woff + ks * 1024;
                bf16x8 wk = tr2(wp, wp + 256);
                bf16x8 wv = tr2(wp + 4096, wp + 4096 + 256);
                bf16x8 wq = tr2(wp + 8192, wp + 8192 + 256);
                aK = MFMA32(wk, xf, aK);
                aV = MFMA32(xf, wv, aV);
                aQ = MFMA32(wq, xf, aQ);
            }
        }
        STAMP(2);
        const u32x4 k0 = sanitize(pack8(aK, 0)), k1 = sanitize(pack8(aK, 8));
        const u32x4 v0 = sanitize(pack8(aV, 0)), v1 = sanitize(pack8(aV, 8));
        const int vo = (r32 >> 4) * 4096 + wave * 1024 + hh * 256 + (r32 & 15) * 16;
        __builtin_amdgcn_raw_buffer_store_b128(k0, wsr, kvbase + wave * 1024 + lane * 16, 0, 16);
        __builtin_amdgcn_raw_buffer_store_b128(k1, wsr, kvbase + 4096 + wave * 1024 + lane * 16, 0, 16);
        __builtin_amdgcn_raw_buffer_store_b128(v0, wsr, kvbase + 8192 + vo, 0, 16);
        __builtin_amdgcn_raw_buffer_store_b128(v1, wsr, kvbase + 8192 + vo + 512, 0, 16);
        *(LAS u32x4*)(lds + L_K + (8 + wave) * 1024 + lane * 16) = k0;
        *(LAS u32x4*)(lds + L_K + 12288 + (8 + wave) * 1024 + lane * 16) = k1;
        const int vl = (r32 >> 4) * 12288 + (8 + wave) * 1024 + hh * 256 + (r32 & 15) * 16;
        *(LAS u32x4*)(lds + L_V + vl) = v0;
        *(LAS u32x4*)(lds + L_V + vl + 512) = v1;
        *(LAS u32x4*)(lds + L_Q + wave * 1024 + lane * 16) = pack8(aQ, 0);
        *(LAS u32x4*)(lds + L_Q + 4096 + wave * 1024 + lane * 16) = pack8(aQ, 8);
        if (wave == 1) {
            for (int spin = 0; spin < SPIN_CAP; ++spin) {
                const bool ok = sflags.x == tag && sflags.y == tag && sflags.z == tag && sflags.w == tag;
                if (__all(ok)) break;
                __builtin_amdgcn_s_sleep(1);
                asm volatile("" ::: "memory");
                sflags = __builtin_amdgcn_raw_buffer_load_b128(wsr, WS_START + lane * 16, 0, 16);
            }
            if (bid == 0 && lane == 0) __builtin_amdgcn_raw_buffer_store_b32(tag, wsr, WS_EPOCH, 0, 16);
        }
    }
    bar();
    STAMP(3);

    const int hd = wave & 1;
    const int qb = (wave < 4) ? 3 - (wave >> 1) : ((wave - 4) >> 1);
    const int orow = g * 256 + (ii >> 3) * 64, ocol = (ii & 7) * 64;
    auto issue_wo = [&]() {
        const int krow = lane >> 3, s16 = lane & 7;
        const int src16 = (((s16 >> 1) ^ ((krow >> 1) & 3)) << 1) | (s16 & 1);
        const u16* p = wo + (size_t)(wave * 64 + krow) * 512 + ocol + src16 * 8;
#pragma unroll
        for (int i = 0; i < 8; ++i) glds16(p + (size_t)i * 8 * 512, lds + L_WO + (wave * 64 + i * 8) * 128);
    };
    if (wave == 4 || wave == 5) {
#pragma unroll
        for (int cc = 0; cc < 2; ++cc) {
            const int c2 = ck - 1 - cc;
            if (c2 >= 0) {
                const unsigned src = WS_KV + (unsigned)(hp * 16 + c2) * 16384u + hd * 4096 + lane * 16;
                u32x4 d[8];
                for (int spin = 0; spin < SPIN_CAP; ++spin) {
                    asm volatile("" ::: "memory");
#pragma unroll
                    for (int i = 0; i < 8; ++i) d[i] = __builtin_amdgcn_raw_buffer_load_b128(wsr, src + (i >> 2) * 8192 + (i & 3) * 1024, 0, 16);
                    unsigned mx = 0;
#pragma unroll
                    for (int i = 0; i < 8; ++i) mx = max(max(mx, max(d[i].x, d[i].y)), max(d[i].z, d[i].w));
                    if (__all(mx != SENT)) break;
                }
#pragma unroll
                for (int i = 0; i < 8; ++i)
                    *(LAS u32x4*)(lds + ((i >> 2) ? L_V : L_K) + hd * 12288 + ((1 - cc) * 4 + (i & 3)) * 1024 + lane * 16) = d[i];
            }
        }
        STAMP(4);
    }
    issue_wo();

    const bf16x8 qf = *(LAS bf16x8*)(lds + L_Q + hd * 4096 + qb * 1024 + lane * 16);
    LAS char* kp = lds + L_K + hd * 12288 + qb * 1024 + lane * 16;
    const int va0 = (r32 < 16) ? (L_V + hd * 12288 + qb * 1024 + hh * 256 + r32 * 16) : (L_ONES + (r32 - 16) * 16);
    const int vstr = (r32 < 16) ? 1024 : 0;
    const int tmin = (ck >= 2) ? 1 : max(1, 8 - 4 * ck - qb);
    const bool has_first = ck >= 2;
    f32x16 O = {};
    float mrun = -__builtin_inff();
    for (int grp = 0; grp < 2; ++grp) {
        const int lo = grp ? tmin : 8 - qb, hi = grp ? 8 - qb : 8;
        if (grp) { bar(); STAMP(5); }
        f32x16 S[9];
        bf16x8 kf[9];
#pragma unroll
        for (int t = 0; t < 9; ++t) kf[t] = *(LAS bf16x8*)(kp + t * 1024);
        const f32x16 zero = {};
#pragma unroll
        for (int t = 1; t < 8; ++t) if (t >= lo && t < hi) S[t] = MFMA32(kf[t], qf, zero);
        if (grp) {
            S[8] = MFMA32(kf[8], qf, zero);
            if (has_first) S[0] = MFMA32(kf[0], qf, zero);
            else {
#pragma unroll
                for (int i = 0; i < 16; ++i) S[0][i] = -__builtin_inff();
            }
#pragma unroll
            for (int i = 0; i < 16; ++i) {
                const int key = (i & 3) + 8 * (i >> 2) + 4 * hh;
                S[0][i] = (key > r32) ? S[0][i] : S[8][i];
            }
        }
        float m = mrun;
#pragma unroll
        for (int t = 0; t < 8; ++t) if (t ? (t >= lo && t < hi) : (grp != 0)) {
#pragma unroll
            for (int i = 0; i < 16; i += 2) m = fmaxf(fmaxf(m, S[t][i]), S[t][i + 1]);
        }
        m = round_bf16(xhalf_max(m));
        if (grp) {
            const float alpha = __builtin_amdgcn_exp2f((mrun - m) * CEXP);
#pragma unroll
            for (int i = 0; i < 9; ++i) O[i] *= alpha;
        }
        mrun = m;
        const float nmc = -m * CEXP;
#pragma unroll
        for (int t = 1; t < 8; ++t) if (t >= lo && t < hi) {
            LAS char* vp = lds + va0 + t * vstr;
            const bf16x8 vf0 = *(LAS bf16x8*)(vp), vf1 = *(LAS bf16x8*)(vp + 512);
            f32x16 e;
#pragma unroll
            for (int i = 0; i < 16; ++i) e[i] = __builtin_amdgcn_exp2f(__builtin_fmaf(round_bf16(S[t][i]), CEXP, nmc));
            O = MFMA32(vf0, as_bf(pack8(e, 0)), O);
            O = MFMA32(vf1, as_bf(pack8(e, 8)), O);
        }
        if (grp) {
            f32x16 e;
#pragma unroll
            for (int i = 0; i < 16; ++i) e[i] = __builtin_amdgcn_exp2f(__builtin_fmaf(round_bf16(S[0][i]), CEXP, nmc));
            const u32x4 p0 = pack8(e, 0), p1 = pack8(e, 8);
            u32x4 mk0, mk1;
#pragma unroll
            for (int j = 0; j < 4; ++j) {
                const int ka = ((2 * j) & 3) + 8 * ((2 * j) >> 2) + 4 * hh;
                mk0[j] = ((ka > r32) ? 0xFFFFu : 0u) | ((ka + 1 > r32) ? 0xFFFF0000u : 0u);
                mk1[j] = ((ka + 16 > r32) ? 0xFFFFu : 0u) | ((ka + 17 > r32) ? 0xFFFF0000u : 0u);
            }
            {
                LAS char* vp = lds + va0 + 8 * vstr;
                const bf16x8 vf0 = *(LAS bf16x8*)(vp), vf1 = *(LAS bf16x8*)(vp + 512);
                O = MFMA32(vf0, as_bf(p0 & ~mk0), O);
                O = MFMA32(vf1, as_bf(p1 & ~mk1), O);
            }
            if (has_first) {
                LAS char* vp = lds + va0;
                const bf16x8 vf0 = *(LAS bf16x8*)(vp), vf1 = *(LAS bf16x8*)(vp + 512);
                O = MFMA32(vf0, as_bf(p0 & mk0), O);
                O = MFMA32(vf1, as_bf(p1 & mk1), O);
            }
        }
    }
    {
        const float rl = 1.0f / O[8];
        f32x16 on;
#pragma unroll
        for (int i = 0; i < 8; ++i) on[i] = O[i] * rl;
        const u32x4 ov = sanitize(pack8(on, 0));
        __builtin_amdgcn_raw_buffer_store_b128(ov, wsr, WS_O + (unsigned)((ck * 4 + qb) * 32 + hp * 2 + hd) * 1024 + lane * 16, 0, 16);
    }
    STAMP(6);
    WAITV(1);
    bar();
    STAMP(7);

    {
        const int ct = wave & 3, th = wave >> 2;
        const unsigned src = WS_O + (unsigned)((orow >> 5) * 32 + wave * 8) * 1024 + lane * 16;
        u32x4 d[8];
#pragma unroll
        for (int i = 0; i < 8; ++i) d[i] = __builtin_amdgcn_raw_buffer_load_b128(wsr, src + i * 1024, 0, 16);
        bf16x8 wf[16];
        {
            const int gq = lane >> 4, q = (lane & 15) >> 2, p = lane & 3;
            const int kr = 16 * (gq >> 1) + 4 * (gq & 1) + q;
            const int a0 = L_WO + kr * 128 + ((ct ^ ((kr >> 1) & 3)) << 5) + p * 8;
#pragma unroll
            for (int s = 0; s < 16; ++s) wf[s] = tr2(lds + a0 + s * 4096, lds + a0 + s * 4096 + 1024);
        }
        for (int spin = 0; spin < SPIN_CAP; ++spin) {
            unsigned mx = 0;
#pragma unroll
            for (int i = 0; i < 8; ++i) mx = max(max(mx, max(d[i].x, d[i].y)), max(d[i].z, d[i].w));
            if (__all(mx != SENT)) break;
            asm volatile("" ::: "memory");
#pragma unroll
            for (int i = 0; i < 8; ++i) d[i] = __builtin_amdgcn_raw_buffer_load_b128(wsr, src + i * 1024, 0, 16);
        }
        STAMP(8);
#pragma unroll
        for (int i = 0; i < 8; ++i) *(LAS u32x4*)(lds + L_OST + (wave * 8 + i) * 1024 + lane * 16) = d[i];
        bar();
        STAMP(9);
        f32x4 c0 = {}, c1 = {};
        const int gq = lane >> 4;
        LAS char* op = lds + L_OST + th * 32768 + (gq >> 1) * 1024 + (gq & 1) * 512 + (lane & 15) * 16;
#pragma unroll
        for (int s = 0; s < 16; ++s) {
            const bf16x8 o0 = *(LAS bf16x8*)(op + s * 2048), o1 = *(LAS bf16x8*)(op + s * 2048 + 256);
            c0 = MFMA16(wf[s], o0, c0);
            c1 = MFMA16(wf[s], o1, c1);
        }
        u16* dst = out + (size_t)(orow + th * 32 + (lane & 15)) * 512 + ocol + ct * 16 + gq * 4;
        u32x2 r0 = {pack2(c0[0], c0[1]), pack2(c0[2], c0[3])}, r1 = {pack2(c1[0], c1[1]), pack2(c1[2], c1[3])};
        *(u32x2*)dst = r0;
        *(u32x2*)(dst + 16 * 512) = r1;
        STAMP(10); CSTAMP(63); STAMP_FLUSH();
    }
}

__global__ void dummy_small(char* ws) { if (ws == nullptr) *(volatile int*)ws = 0; }
__global__ __launch_bounds__(512, 2) void dummy_big(char* ws) {
    extern __shared__ __attribute__((aligned(16))) char sm2[];
    asm volatile("v_mov_b32 v211, 0" ::: "v211");
    if (ws == nullptr) *(volatile int*)ws = sm2[threadIdx.x];
}
__global__ __launch_bounds__(256, 1) void dummy_mid(char* ws) {
    extern __shared__ __attribute__((aligned(16))) char sm2[];
    if (ws == nullptr) *(volatile int*)ws = sm2[threadIdx.x];
}

extern "C" void kernel_launch(void* const* d_in, const int* in_sizes, int n_in, void* d_out, int out_size, void* d_ws,
                              size_t ws_size, hipStream_t stream) {
    (void)in_sizes; (void)n_in; (void)out_size; (void)ws_size;
    (void)hipFuncSetAttribute((const void*)swa_block, hipFuncAttributeMaxDynamicSharedMemorySize, LDS_BYTES);
    swa_block<<<256, 512, LDS_BYTES, stream>>>((const u16*)d_in[0], (const u16*)d_in[1], (const u16*)d_in[2], (u16*)d_out, (char*)d_ws);
    dummy_mid<<<256, 256, 0, stream>>>((char*)d_ws);
}
```

```cpp
#include <hip/hip_runtime.h>
#include <stdint.h>

typedef short bf16x8 __attribute__((ext_vector_type(8)));
typedef short s16x4 __attribute__((ext_vector_type(4)));
typedef float f32x16 __attribute__((ext_vector_type(16)));
typedef float f32x4 __attribute__((ext_vector_type(4)));
typedef float f32x2 __attribute__((ext_vector_type(2)));
typedef unsigned u32x4 __attribute__((ext_vector_type(4)));
typedef unsigned u32x2 __attribute__((ext_vector_type(2)));
typedef __bf16 bf16x2 __attribute__((ext_vector_type(2)));
typedef unsigned short u16;
#define LAS __attribute__((address_space(3)))
#define GAS __attribute__((address_space(1)))
#define DI __device__ __forceinline__

#define KH_POS 28
#define VH_POS 36
#define CLR_POS 8
#define DEPTH 5

constexpr unsigned WS_EPOCH = 0;
constexpr unsigned WS_START = 1024;
constexpr unsigned WS_K = 65536;
constexpr unsigned WS_V = WS_K + 32 * 64 * 1024;
constexpr unsigned WS_O = WS_V + 32 * 64 * 1024;
constexpr unsigned WS_END = WS_O + 64 * 32 * 1024;
constexpr int L_RING = 0;
constexpr int L_K = 65536;
constexpr int L_V = L_K + 40960;
constexpr int L_Q = L_V + 40960;
constexpr int L_ONES = L_Q + 8192;
constexpr int L_FULL = L_ONES + 1024;
constexpr int L_CONS = L_FULL + 16;
constexpr int L_WO = 0;
constexpr int L_OS = L_K;
constexpr int L_SOK = L_CONS + 16;
constexpr int LDS_BYTES = L_SOK + 16;

constexpr float CEXP = 0.36067376022224085f;
constexpr unsigned SENT = 0xFFFFFFFFu;
constexpr int SPIN_CAP = 1 << 18;

DI unsigned pack2(float a, float b) {
    f32x2 v = {a, b};
    return __builtin_bit_cast(unsigned, __builtin_convertvector(v, bf16x2));
}
DI u32x4 pack8(const f32x16& a, int o) {
    u32x4 r = {pack2(a[o], a[o + 1]), pack2(a[o + 2], a[o + 3]), pack2(a[o + 4], a[o + 5]), pack2(a[o + 6], a[o + 7])};
    return r;
}
DI u32x4 sanitize(u32x4 v) {
    u32x4 r = {min(v.x, SENT - 1), min(v.y, SENT - 1), min(v.z, SENT - 1), min(v.w, SENT - 1)};
    return r;
}
DI bf16x8 as_bf(u32x4 v) { return __builtin_bit_cast(bf16x8, v); }
DI u32x4 as_u(bf16x8 v) { return __builtin_bit_cast(u32x4, v); }
DI void bar() { asm volatile("s_waitcnt lgkmcnt(0)" ::: "memory"); __builtin_amdgcn_s_barrier(); asm volatile("" ::: "memory"); }
template <int N> DI void waitv() { asm volatile("s_waitcnt vmcnt(%0)" :: "i"(N) : "memory"); }
#define CFENCE() asm volatile("" ::: "memory")
#define SB() __builtin_amdgcn_sched_barrier(0)
#define MFMA32(a, b, c) __builtin_amdgcn_mfma_f32_32x32x16_bf16((a), (b), (c), 0, 0, 0)
#define MFMA16(a, b, c) __builtin_amdgcn_mfma_f32_16x16x32_bf16((a), (b), (c), 0, 0, 0)
DI s16x4 trread(LAS char* p) { return __builtin_amdgcn_ds_read_tr16_b64_v4i16((LAS s16x4*)p); }
DI bf16x8 tr2(LAS char* p0, LAS char* p1) {
    s16x4 a = trread(p0), b = trread(p1);
    return __builtin_shufflevector(a, b, 0, 1, 2, 3, 4, 5, 6, 7);
}
DI void glds16(const void* sbase, unsigned voff, unsigned ldsaddr) {
    asm volatile("s_mov_b32 m0, %0\n\ts_nop 0\n\tglobal_load_lds_dwordx4 %1, %2" :: "s"(ldsaddr), "v"(voff), "s"(sbase) : "memory");
}
DI void blds16_sc1(__amdgpu_buffer_rsrc_t rs, unsigned voff, unsigned soff, unsigned ldsaddr) {
    asm volatile("s_mov_b32 m0, %0\n\ts_nop 0\n\tbuffer_load_dwordx4 %1, %2, %3 offen sc1 lds" :: "s"(ldsaddr), "v"(voff), "s"(rs), "s"(soff) : "memory");
}
DI float xhalf_max(float m) {
    auto r = __builtin_amdgcn_permlane32_swap(__float_as_uint(m), __float_as_uint(m), false, false);
    return fmaxf(__uint_as_float(r[0]), __uint_as_float(r[1]));
}
template <int N> DI bool all_valid(const u32x4 (&d)[N]) {
    unsigned mx = 0;
#pragma unroll
    for (int i = 0; i < N; ++i) mx = max(max(mx, max(d[i].x, d[i].y)), max(d[i].z, d[i].w));
    return __all(mx != SENT);
}
DI unsigned min4(u32x4 f) { return __builtin_amdgcn_readfirstlane(min(min(f.x, f.y), min(f.z, f.w))); }
DI u32x4 ldflag(LAS char* p) { CFENCE(); u32x4 f = *(volatile LAS u32x4*)p; CFENCE(); return f; }
DI void wait_ge(LAS char* p, unsigned n) {
    for (int spin = 0; spin < SPIN_CAP; ++spin) if (min4(ldflag(p)) >= n) break;
}
DI void stflag(LAS char* p, unsigned v) { CFENCE(); *(volatile LAS unsigned*)p = v; CFENCE(); }

#define STAMP(i) do {} while (0)
#define CSTAMP(i) do {} while (0)
#define STAMP_FLUSH() do {} while (0)

DI void refetch_halo(__amdgpu_buffer_rsrc_t wsr, unsigned src, int nskip, LAS char* dst) {
    u32x4 d[8];
#pragma unroll 1
    for (int spin = 0; spin < SPIN_CAP; ++spin) {
        CFENCE();
#pragma unroll
        for (int i = 0; i < 8; ++i) d[i] = __builtin_amdgcn_raw_buffer_load_b128(wsr, src, i >= nskip ? i * 1024 : 0x40000000, 16);
        if (all_valid(d)) break;
    }
#pragma unroll
    for (int i = 0; i < 8; ++i) *(LAS u32x4*)(dst + i * 1024) = d[i];
    asm volatile("s_waitcnt lgkmcnt(0)" ::: "memory");
}

__global__ __launch_bounds__(512, 2) void swa_block(const u16* __restrict__ x, const u16* __restrict__ wqkv,
                                                    const u16* __restrict__ wo, u16* __restrict__ out, char* ws) {
    extern __shared__ __attribute__((aligned(16))) char smem_[];
    LAS char* lds = (LAS char*)smem_;
    const int tid = threadIdx.x;
    const int wave = __builtin_amdgcn_readfirstlane(tid >> 6);
    const int lane = tid & 63, r32 = lane & 31, hh = lane >> 5;
    const int bid = blockIdx.x, g = bid & 7, ii = bid >> 3;
    const int hg = (g & 1) * 4 + (ii & 3);
    const int ck = (g >> 1) * 8 + (ii >> 2);
    const int rot = ((ii >> 2) + 2 * (ii & 3)) & 7;
    const int orow = g * 256 + (ii >> 3) * 64, ocol = (ii & 7) * 64;
    const __amdgpu_buffer_rsrc_t wsr = __builtin_amdgcn_make_buffer_rsrc(ws, 0, WS_END, 0x00020000);
    const unsigned lbase = (unsigned)(uintptr_t)lds;
    const int hbase = 2 * ck - 8;
    const int nskip = hbase < 0 ? -hbase : 0;
    const int q4 = (lane & 15) >> 2;
    unsigned stampv = 0; (void)stampv;
    STAMP(0); CSTAMP(62);

    if (wave >= 4) {
        const int lw = wave - 4;
        const unsigned xvo = (unsigned)((lane >> 3) * 512 + (((lane & 7) ^ (lane >> 4) ^ ((lw & 1) << 2)) * 8)) * 2u;
        const unsigned wvo = (unsigned)((lane >> 3) * 1536 + ((((lane >> 2) & 1) ^ ((lane >> 4) & 1)) * 4 + (lane & 3)) * 8) * 2u;
        const u16* xsrc = x + (size_t)(ck * 64 + lw * 8) * 512;
        const u16* wsrc = wqkv + (size_t)(lw * 8) * 1536 + hg * 64;
        const unsigned hvo = (unsigned)((int)WS_K + ((hg * 4 + lw) * 64 + hbase) * 1024 + lane * 16);
        auto issue = [&](int n) {
            const int s = n < 16 ? (n >> 1) : (n & 7);
            const int kb = (s + rot) & 7;
            const unsigned dst = lbase + L_RING + (n & 7) * 8192 + lw * 1024;
            if (n < 16 && !(n & 1)) {
                glds16(xsrc + kb * 64, xvo, dst);
                glds16(xsrc + kb * 64 + 32 * 512, xvo, dst + 4096);
            } else {
                const u16* wp = wsrc + (size_t)kb * 64 * 1536 + (n < 16 ? 512 : n < 24 ? 1024 : 0);
                glds16(wp, wvo, dst);
                glds16(wp + 32 * 1536, wvo, dst + 4096);
            }
        };
        issue(0); issue(1);
#pragma unroll
        for (int n = 0; n < 32; ++n) {
            const int m = (n + DEPTH < 2 * n + 2) ? n + DEPTH : 2 * n + 2;
            const int mp = n == 0 ? 1 : ((n - 1 + DEPTH < 2 * n) ? n - 1 + DEPTH : 2 * n);
#pragma unroll
            for (int j = mp + 1; j <= m; ++j) if (j < 32) issue(j);
            if (m == KH_POS) {
#pragma unroll
                for (int i = 0; i < 8; ++i) blds16_sc1(wsr, hvo, i >= nskip ? i * 1024 : 0x40000000, lbase + L_K + lw * 10240 + i * 1024);
            }
            if (m == VH_POS) {
#pragma unroll
                for (int i = 0; i < 8; ++i) blds16_sc1(wsr, hvo + (WS_V - WS_K), i >= nskip ? i * 1024 : 0x40000000, lbase + L_V + lw * 10240 + i * 1024);
            }
            switch (2 * ((m < 32 ? m : 31) - n) + ((KH_POS >= n && KH_POS <= m) ? 8 : 0) + ((VH_POS >= n && VH_POS <= m) ? 8 : 0)) {
#define WC(k) case k: waitv<k>(); break;
                WC(0) WC(2) WC(4) WC(6) WC(8) WC(10) WC(12) WC(14) WC(16) WC(18) WC(20) WC(22) WC(24) WC(26) WC(28)
#undef WC
                default: waitv<0>();
            }
            __builtin_amdgcn_s_barrier();
        }
        STAMP(16);
    } else {
        const int ft = wave >> 1, tb = wave & 1;
        unsigned touch;
        {
            const u16* tp = wave < 2 ? wqkv + (size_t)(rot * 64 + lane) * 1536 + hg * 64 + (wave == 0 ? 1024 : 0)
                                     : wo + (size_t)((ii >> 3) * 128 + (wave - 2) * 64 + lane) * 512 + ocol;
            asm volatile("global_load_dword %0, %1, off" : "=v"(touch) : "v"(tp) : "memory");
            const u32x4 sv = {SENT, SENT, SENT, SENT};
#pragma unroll
            for (int b = 0; b < 2; ++b) {
                const unsigned hb = (unsigned)((hg * 4 + wave) * 64 + ck * 2 + b) * 1024 + lane * 16;
                __builtin_amdgcn_raw_buffer_store_b128(sv, wsr, WS_K + hb, 0, 16);
                __builtin_amdgcn_raw_buffer_store_b128(sv, wsr, WS_V + hb, 0, 16);
                __builtin_amdgcn_raw_buffer_store_b128(sv, wsr, WS_O + (unsigned)((ck * 2 + b) * 32 + hg * 4 + wave) * 1024 + lane * 16, 0, 16);
            }
        }
        if (wave == 0) {
            const u32x4 one = {0x3F803F80u, 0x3F803F80u, 0x3F803F80u, 0x3F803F80u};
            *(LAS u32x4*)(lds + L_ONES + lane * 16) = one;
        }
        unsigned tag;
        asm volatile("s_load_dword %0, %1, 0x0\n\ts_waitcnt lgkmcnt(0)" : "=s"(tag) : "s"(ws) : "memory");
        tag += 1u;
        int xoff[4];
        {
            const int u = hh ^ ((r32 >> 1) & 7);
#pragma unroll
            for (int ks = 0; ks < 4; ++ks) xoff[ks] = L_RING + (tb * 32 + r32) * 128 + (((2 * ks) ^ u) << 4);
        }
        const int woff = L_RING + (8 * hh + q4) * 128 + ((ft ^ (q4 >> 1)) << 6) + ((lane >> 4) & 1) * 32 + (lane & 3) * 8;
        const unsigned kb0 = (unsigned)((hg * 4 + 2 * ft) * 64 + ck * 2 + tb) * 1024;
        bf16x8 xf[32], wf[2][4];
        f32x16 acc = {};
        const bool sneed = lane < 4 ? (ck - 1 - lane >= 0) : lane < 12;
        const unsigned soff = WS_START + (unsigned)(!sneed ? hg * 32 + ck : lane < 4 ? hg * 32 + ck - 1 - lane : (lane - 4) * 32 + (orow >> 6)) * 128;
        unsigned sflag = 0;
#pragma unroll
        for (int n = 0; n <= 32; ++n) {
            if (n < 32) {
                if (n == CLR_POS) { waitv<0>(); asm volatile("" :: "v"(touch)); }
                bar();
                if ((n & 1) == 0) STAMP(30 + (n >> 1));
                if (n < 16 && !(n & 1)) {
                    LAS char* xb = lds + (n & 7) * 8192;
#pragma unroll
                    for (int ks = 0; ks < 4; ++ks) xf[2 * n + ks] = *(LAS bf16x8*)(xb + xoff[ks]);
                } else {
                    LAS char* wb = lds + woff + (n & 7) * 8192;
#pragma unroll
                    for (int ks = 0; ks < 4; ++ks) wf[n & 1][ks] = tr2(wb + ks * 2048, wb + ks * 2048 + 512);
                }
                if (n == CLR_POS && tid == 0) __builtin_amdgcn_raw_buffer_store_b32(tag, wsr, WS_START + (hg * 32 + ck) * 128, 0, 16);
                if (n == 15 && wave == 0) sflag = __builtin_amdgcn_raw_buffer_load_b32(wsr, soff, 0, 16);
            }
            SB();
            const int p = n - 1;
            if (p >= 1 && (p >= 16 || (p & 1))) {
                const int s = p < 16 ? (p >> 1) : (p & 7);
                if (p == 16 || p == 24) acc = f32x16{};
#pragma unroll
                for (int ks = 0; ks < 4; ++ks) {
                    if (p >= 16 && p < 24) acc = MFMA32(xf[4 * s + ks], wf[p & 1][ks], acc);
                    else acc = MFMA32(wf[p & 1][ks], xf[4 * s + ks], acc);
                }
            }
            SB();
            if (p == 15) {
                const u32x4 k0 = sanitize(pack8(acc, 0)), k1 = sanitize(pack8(acc, 8));
                __builtin_amdgcn_raw_buffer_store_b128(k0, wsr, WS_K + kb0 + lane * 16, 0, 16);
                __builtin_amdgcn_raw_buffer_store_b128(k1, wsr, WS_K + kb0 + 65536 + lane * 16, 0, 16);
                *(LAS u32x4*)(lds + L_K + (2 * ft) * 10240 + (8 + tb) * 1024 + lane * 16) = k0;
                *(LAS u32x4*)(lds + L_K + (2 * ft + 1) * 10240 + (8 + tb) * 1024 + lane * 16) = k1;
                STAMP(2);
            }
            if (n == KH_POS - DEPTH - 2 && wave == 0) {
                waitv<2>();
#pragma unroll 1
                for (int spin = 0; spin < SPIN_CAP; ++spin) {
                    if (__all(sflag == tag)) break;
                    __builtin_amdgcn_s_sleep(1);
                    CFENCE();
                    sflag = __builtin_amdgcn_raw_buffer_load_b32(wsr, soff, 0, 16);
                }
            }
            if (p == 23) {
                const u32x4 v0 = sanitize(pack8(acc, 0)), v1 = sanitize(pack8(acc, 8));
                const int vsub = hh * 256 + (r32 & 15) * 16;
                __builtin_amdgcn_raw_buffer_store_b128(v0, wsr, WS_V + kb0 + (r32 >> 4) * 65536 + vsub, 0, 16);
                __builtin_amdgcn_raw_buffer_store_b128(v1, wsr, WS_V + kb0 + (r32 >> 4) * 65536 + vsub + 512, 0, 16);
                const int vl = L_V + (2 * ft + (r32 >> 4)) * 10240 + (8 + tb) * 1024 + vsub;
                *(LAS u32x4*)(lds + vl) = v0;
                *(LAS u32x4*)(lds + vl + 512) = v1;
                STAMP(3);
            }
        }
        *(LAS u32x4*)(lds + L_Q + (2 * ft) * 2048 + tb * 1024 + lane * 16) = pack8(acc, 0);
        *(LAS u32x4*)(lds + L_Q + (2 * ft + 1) * 2048 + tb * 1024 + lane * 16) = pack8(acc, 8);
        STAMP(4);
    }
    bar();
    STAMP(5);

    const int hd = wave & 3, qb = wave >> 2;
    {
        const int krow = lane >> 3, s16 = lane & 7;
        const int src16 = (((s16 >> 1) ^ ((krow >> 1) & 3)) << 1) | (s16 & 1);
        const u16* p = wo + (size_t)(wave * 64) * 512 + ocol;
        const unsigned vo = (unsigned)(krow * 512 + src16 * 8) * 2u;
#pragma unroll
        for (int i = 0; i < 8; ++i) glds16(p + (size_t)i * 8 * 512, vo, lbase + L_WO + wave * 8192 + i * 1024);
    }
    const unsigned hsrc = (unsigned)((int)WS_K + ((hg * 4 + hd) * 64 + hbase) * 1024 + lane * 16);
    u32x4 ov;
#pragma unroll 1
    for (int attempt = 0; attempt < 2; ++attempt) {
        const bf16x8 qf = *(LAS bf16x8*)(lds + L_Q + hd * 2048 + qb * 1024 + lane * 16);
        LAS char* kp = lds + L_K + hd * 10240 + qb * 1024 + lane * 16;
        bf16x8 kf[9];
#pragma unroll 1
        for (int tryk = 0; tryk < 2; ++tryk) {
            unsigned mx = 0;
#pragma unroll
            for (int t = 0; t < 9; ++t) { kf[t] = *(LAS bf16x8*)(kp + t * 1024); mx = max(mx, as_u(kf[t]).w); }
            if (__all(mx != SENT) && !(attempt && !tryk)) break;
            refetch_halo(wsr, hsrc, nskip, lds + L_K + hd * 10240 + lane * 16);
        }
        u32x4 qh, ql;
        {
            const u32x4 qu = as_u(qf);
#pragma unroll
            for (int j = 0; j < 4; ++j) {
                const float a = __uint_as_float(qu[j] << 16) * CEXP, b = __uint_as_float(qu[j] & 0xFFFF0000u) * CEXP;
                qh[j] = pack2(a, b);
                ql[j] = pack2(a - __uint_as_float(qh[j] << 16), b - __uint_as_float(qh[j] & 0xFFFF0000u));
            }
        }
        const f32x16 zero = {};
        const float NINF = -__builtin_inff();
        float m;
        {
            f32x16 Sa = MFMA32(kf[0], qf, zero), Sb = MFMA32(kf[8], qf, zero);
            f32x16 S0 = MFMA32(kf[1], qf, zero), S1;
            if (nskip > 0) {
                if (qb < nskip) {
#pragma unroll
                    for (int i = 0; i < 16; ++i) Sa[i] = NINF;
                }
                asm volatile("" : "+v"(Sa));
            }
#pragma unroll
            for (int i = 0; i < 16; ++i) { const int key = (i & 3) + 8 * (i >> 2) + 4 * hh; Sa[i] = (key > r32) ? Sa[i] : Sb[i]; }
            m = Sa[0];
#pragma unroll
            for (int i = 0; i < 16; i += 2) m = fmaxf(fmaxf(m, Sa[i]), Sa[i + 1]);
#pragma unroll
            for (int t = 1; t < 8; ++t) {
                f32x16& Sc = (t & 1) ? S0 : S1;
                f32x16& Sn = (t & 1) ? S1 : S0;
                if (t + 1 < 8) Sn = MFMA32(kf[t + 1], qf, zero);
                SB();
                float mt = fmaxf(Sc[0], Sc[1]);
#pragma unroll
                for (int i = 2; i < 16; i += 2) mt = fmaxf(fmaxf(mt, Sc[i]), Sc[i + 1]);
                m = fmaxf(m, (qb + t < nskip) ? m : mt);
                SB();
            }
        }
        m = xhalf_max(m);
        f32x16 cn;
        {
            const float nmc = -m * CEXP;
#pragma unroll
            for (int i = 0; i < 16; ++i) cn[i] = nmc;
        }
        f32x16 T0 = MFMA32(kf[1], as_bf(qh), cn), T1;
        T0 = MFMA32(kf[1], as_bf(ql), T0);
        STAMP(11);
        if (!attempt) { if (wave >= 4) waitv<8>(); bar(); }
        STAMP(12);
        const int va0 = (r32 < 16) ? (L_V + hd * 10240 + qb * 1024 + hh * 256 + r32 * 16) : (L_ONES + (r32 - 16) * 16);
        const int vstr = (r32 < 16) ? 1024 : 0;
        {
            unsigned mx = 0;
#pragma unroll
            for (int t = 0; t < 8; ++t) mx = max(mx, *(LAS unsigned*)(lds + L_V + hd * 10240 + t * 1024 + lane * 16 + 12));
            if (attempt || !__all(mx != SENT)) refetch_halo(wsr, hsrc + (WS_V - WS_K), nskip, lds + L_V + hd * 10240 + lane * 16);
        }
        f32x16 O = {};
        f32x16 Tb;
#pragma unroll
        for (int t = 1; t < 8; ++t) {
            f32x16& Tc = (t & 1) ? T0 : T1;
            f32x16& Tn = (t & 1) ? T1 : T0;
            LAS char* vp = lds + va0 + t * vstr;
            const bf16x8 vf0 = *(LAS bf16x8*)(vp), vf1 = *(LAS bf16x8*)(vp + 512);
            const int tn = (t + 1 < 8) ? t + 1 : 0;
            Tn = MFMA32(kf[tn], as_bf(qh), cn);
            Tn = MFMA32(kf[tn], as_bf(ql), Tn);
            if (t == 7) { Tb = MFMA32(kf[8], as_bf(qh), cn); Tb = MFMA32(kf[8], as_bf(ql), Tb); }
            SB();
            if (nskip > 0) {
                if (qb + t < nskip) {
#pragma unroll
                    for (int i = 0; i < 16; ++i) Tc[i] = NINF;
                }
                asm volatile("" : "+v"(Tc));
            }
#pragma unroll
            for (int i = 0; i < 16; ++i) Tc[i] = __builtin_amdgcn_exp2f(Tc[i]);
            O = MFMA32(vf0, as_bf(pack8(Tc, 0)), O);
            O = MFMA32(vf1, as_bf(pack8(Tc, 8)), O);
            SB();
        }
        {
            f32x16& e = T1;
            LAS char* vp = lds + va0 + 8 * vstr;
            const bf16x8 vf0 = *(LAS bf16x8*)(vp), vf1 = *(LAS bf16x8*)(vp + 512);
            LAS char* vq = lds + va0;
            const bf16x8 vg0 = *(LAS bf16x8*)(vq), vg1 = *(LAS bf16x8*)(vq + 512);
            if (nskip > 0) {
                if (qb < nskip) {
#pragma unroll
                    for (int i = 0; i < 16; ++i) e[i] = NINF;
                }
                asm volatile("" : "+v"(e));
            }
#pragma unroll
            for (int i = 0; i < 16; ++i) {
                const int key = (i & 3) + 8 * (i >> 2) + 4 * hh;
                e[i] = __builtin_amdgcn_exp2f((key > r32) ? e[i] : Tb[i]);
            }
            const u32x4 p0 = pack8(e, 0), p1 = pack8(e, 8);
            u32x4 mk0, mk1;
#pragma unroll
            for (int j = 0; j < 4; ++j) {
                const int ka = ((2 * j) & 3) + 8 * ((2 * j) >> 2) + 4 * hh;
                mk0[j] = ((ka > r32) ? 0xFFFFu : 0u) | ((ka + 1 > r32) ? 0xFFFF0000u : 0u);
                mk1[j] = ((ka + 16 > r32) ? 0xFFFFu : 0u) | ((ka + 17 > r32) ? 0xFFFF0000u : 0u);
            }
            O = MFMA32(vf0, as_bf(p0 & ~mk0), O);
            O = MFMA32(vf1, as_bf(p1 & ~mk1), O);
            O = MFMA32(vg0, as_bf(p0 & mk0), O);
            O = MFMA32(vg1, as_bf(p1 & mk1), O);
        }
        const float rl = __builtin_amdgcn_rcpf(O[8]);
        f32x16 on;
        float chk = 0.f;
#pragma unroll
        for (int i = 0; i < 8; ++i) { on[i] = O[i] * rl; chk += on[i]; }
        ov = sanitize(pack8(on, 0));
        if (__all(chk == chk)) break;
    }
    __builtin_amdgcn_raw_buffer_store_b128(ov, wsr, WS_O + (unsigned)((ck * 2 + qb) * 32 + hg * 4 + hd) * 1024 + lane * 16, 0, 16);
    STAMP(6);
    waitv<1>();
    bar();
    STAMP(7);

    {
        const int ct = wave & 3, th = wave >> 2;
        const unsigned src = WS_O + (unsigned)((orow >> 5) * 32 + wave * 8) * 1024 + lane * 16;
        u32x4 d[8];
#pragma unroll
        for (int i = 0; i < 8; ++i) d[i] = __builtin_amdgcn_raw_buffer_load_b128(wsr, src + i * 1024, 0, 16);
        bf16x8 wf[16];
        {
            const int gq = lane >> 4, p = lane & 3;
            const int kr = 16 * (gq >> 1) + 4 * (gq & 1) + q4;
            const int a0 = L_WO + kr * 128 + ((ct ^ ((kr >> 1) & 3)) << 5) + p * 8;
#pragma unroll
            for (int s = 0; s < 16; ++s) wf[s] = tr2(lds + a0 + s * 4096, lds + a0 + s * 4096 + 1024);
        }
#pragma unroll 1
        for (int spin = 0; spin < SPIN_CAP; ++spin) {
            if (all_valid(d)) break;
            CFENCE();
#pragma unroll
            for (int i = 0; i < 8; ++i) d[i] = __builtin_amdgcn_raw_buffer_load_b128(wsr, src + i * 1024, 0, 16);
        }
        STAMP(8);
#pragma unroll
        for (int i = 0; i < 8; ++i) *(LAS u32x4*)(lds + L_OS + (wave * 8 + i) * 1024 + lane * 16) = d[i];
        bar();
        STAMP(9);
        f32x4 c0 = {}, c1 = {};
        const int gq = lane >> 4;
        LAS char* op = lds + L_OS + th * 32768 + (gq >> 1) * 1024 + (gq & 1) * 512 + (lane & 15) * 16;
#pragma unroll
        for (int s = 0; s < 16; ++s) {
            const bf16x8 o0 = *(LAS bf16x8*)(op + s * 2048), o1 = *(LAS bf16x8*)(op + s * 2048 + 256);
            c0 = MFMA16(wf[s], o0, c0);
            c1 = MFMA16(wf[s], o1, c1);
        }
        u16* dst = out + (size_t)(orow + th * 32 + (lane & 15)) * 512 + ocol + ct * 16 + gq * 4;
        u32x2 r0 = {pack2(c0[0], c0[1]), pack2(c0[2], c0[3])}, r1 = {pack2(c1[0], c1[1]), pack2(c1[2], c1[3])};
        *(u32x2*)dst = r0;
        *(u32x2*)(dst + 16 * 512) = r1;
        if (bid == 0 && wave == 7) {
            unsigned tg;
            asm volatile("s_load_dword %0, %1, 0x0\n\ts_waitcnt lgkmcnt(0)" : "=s"(tg) : "s"(ws) : "memory");
            tg += 1u;
#pragma unroll 1
            for (int spin = 0; spin < SPIN_CAP; ++spin) {
                CFENCE();
                bool ok = true;
#pragma unroll
                for (int i = 0; i < 4; ++i) ok &= __builtin_amdgcn_raw_buffer_load_b32(wsr, WS_START + (i * 64 + lane) * 128, 0, 16) == tg;
                if (__all(ok)) break;
            }
            if (lane == 0) __builtin_amdgcn_raw_buffer_store_b32(tg, wsr, WS_EPOCH, 0, 16);
        }
        STAMP(10); CSTAMP(63); STAMP_FLUSH();
    }
}

extern "C" void kernel_launch(void* const* d_in, const int* in_sizes, int n_in, void* d_out, int out_size, void* d_ws,
                              size_t ws_size, hipStream_t stream) {
    (void)in_sizes; (void)n_in; (void)out_size; (void)ws_size;
    (void)hipFuncSetAttribute((const void*)swa_block, hipFuncAttributeMaxDynamicSharedMemorySize, LDS_BYTES);
    swa_block<<<256, 512, LDS_BYTES, stream>>>((const u16*)d_in[0], (const u16*)d_in[1], (const u16*)d_in[2], (u16*)d_out, (char*)d_ws);
}
```

```cpp
#include <hip/hip_runtime.h>
#include <stdint.h>
#include <type_traits>

typedef short bf16x8 __attribute__((ext_vector_type(8)));
typedef short s16x4 __attribute__((ext_vector_type(4)));
typedef float f32x16 __attribute__((ext_vector_type(16)));
typedef float f32x4 __attribute__((ext_vector_type(4)));
typedef float f32x2 __attribute__((ext_vector_type(2)));
typedef unsigned u32x4 __attribute__((ext_vector_type(4)));
typedef unsigned u32x2 __attribute__((ext_vector_type(2)));
typedef __bf16 bf16x2 __attribute__((ext_vector_type(2)));
typedef unsigned short u16;
typedef unsigned long long u64;
#define LAS __attribute__((address_space(3)))
#define DI __device__ __forceinline__

#define AFLAG_POS 6
#define PROBE_SLEEP 2
#define CLR_MODE 1
#define GAP8 0
#define KH_EARLY 1

constexpr unsigned WS_EPOCH = 0;
constexpr unsigned WS_FLAG = 1024;
constexpr unsigned WS_K = 65536;
constexpr unsigned WS_V = WS_K + 32 * 64 * 1024;
constexpr unsigned WS_O = WS_V + 32 * 64 * 1024;
constexpr unsigned WS_END = WS_O + 64 * 32 * 1024;
constexpr int L_RING = 0;
constexpr int L_Q = 32768;
constexpr int L_V = 40960;
constexpr int L_K = 114688;
constexpr int L_ONES = L_K + 40960;
constexpr int L_FL = L_ONES + 1024;
constexpr int L_FG = L_FL + 16;
constexpr int L_RED = 32768;
constexpr int LDS_BYTES = L_FG + 16;
constexpr int slot_of(int n) { return (n % 7) * 16384; }

constexpr float CEXP = 0.36067376022224085f;
constexpr float HEADROOM = 32.f;
constexpr unsigned SENT = 0xFFFFFFFFu;
constexpr unsigned OOB = 0x40000000u;
constexpr int SPIN_CAP = 1 << 18;

DI unsigned pack2(float a, float b) {
    f32x2 v = {a, b};
    return __builtin_bit_cast(unsigned, __builtin_convertvector(v, bf16x2));
}
DI u32x4 pack8(const f32x16& a, int o) {
    u32x4 r = {pack2(a[o], a[o + 1]), pack2(a[o + 2], a[o + 3]), pack2(a[o + 4], a[o + 5]), pack2(a[o + 6], a[o + 7])};
    return r;
}
DI u32x4 sanitize(u32x4 v) {
    v.w = min(v.w, SENT - 1);
    return v;
}
DI bf16x8 as_bf(u32x4 v) { return __builtin_bit_cast(bf16x8, v); }
DI u32x4 as_u(bf16x8 v) { return __builtin_bit_cast(u32x4, v); }
DI void waitl0() { asm volatile("" ::: "memory"); __builtin_amdgcn_s_waitcnt(0xC07F); asm volatile("" ::: "memory"); }
DI void bar() { waitl0(); __builtin_amdgcn_s_barrier(); asm volatile("" ::: "memory"); }
template <int N> DI void waitv() { asm volatile("s_waitcnt vmcnt(%0)" :: "i"(N) : "memory"); }
#define CFENCE() asm volatile("" ::: "memory")
#define SB() __builtin_amdgcn_sched_barrier(0)
#define PIN(x) asm volatile("" : "+v"(x))
#define MFMA32(a, b, c) __builtin_amdgcn_mfma_f32_32x32x16_bf16((a), (b), (c), 0, 0, 0)
DI s16x4 trread(LAS char* p) { return __builtin_amdgcn_ds_read_tr16_b64_v4i16((LAS s16x4*)p); }
DI bf16x8 tr2(LAS char* p0, LAS char* p1) {
    s16x4 a = trread(p0), b = trread(p1);
    return __builtin_shufflevector(a, b, 0, 1, 2, 3, 4, 5, 6, 7);
}
DI void glds16(const void* sbase, unsigned voff, unsigned ldsaddr) {
    asm volatile("s_mov_b32 m0, %0\n\ts_nop 0\n\tglobal_load_lds_dwordx4 %1, %2" :: "s"(ldsaddr), "v"(voff), "s"(sbase) : "memory");
}
DI void blds16_sc1(__amdgpu_buffer_rsrc_t rs, unsigned voff, unsigned soff, unsigned ldsaddr) {
    asm volatile("s_mov_b32 m0, %0\n\ts_nop 0\n\tbuffer_load_dwordx4 %1, %2, %3 offen sc1 lds" :: "s"(ldsaddr), "v"(voff), "s"(rs), "s"(soff) : "memory");
}
DI float xhalf_max(float m) {
    auto r = __builtin_amdgcn_permlane32_swap(__float_as_uint(m), __float_as_uint(m), false, false);
    return fmaxf(__uint_as_float(r[0]), __uint_as_float(r[1]));
}
DI unsigned min4(u32x4 f) { return __builtin_amdgcn_readfirstlane(min(min(f.x, f.y), min(f.z, f.w))); }
DI u32x4 ldflag4(LAS char* p) { CFENCE(); u32x4 f = *(volatile LAS u32x4*)p; CFENCE(); return f; }
DI unsigned ldflag1(LAS char* p) { CFENCE(); unsigned f = *(volatile LAS unsigned*)p; CFENCE(); return __builtin_amdgcn_readfirstlane(f); }
#define UNLIKELY(c) __builtin_expect(!!(c), 0)
#define AINL __attribute__((always_inline))
template <int V> using IC = std::integral_constant<int, V>;
DI void wait4_ge(LAS char* p, unsigned n) {
    if (UNLIKELY(min4(ldflag4(p)) < n)) {
#pragma unroll 1
        for (int spin = 0; spin < SPIN_CAP; ++spin) if (min4(ldflag4(p)) >= n) break;
    }
}
DI void wait1_ge(LAS char* p, unsigned n) {
    if (UNLIKELY(ldflag1(p) < n)) {
#pragma unroll 1
        for (int spin = 0; spin < SPIN_CAP; ++spin) if (ldflag1(p) >= n) break;
    }
}
DI void blds4k_sc1(__amdgpu_buffer_rsrc_t rs, unsigned voff, unsigned soff, unsigned ldsaddr) {
    asm volatile("s_mov_b32 m0, %0\n\ts_nop 0\n\tbuffer_load_dwordx4 %1, %2, %3 offen sc1 lds\n\t"
                 "buffer_load_dwordx4 %1, %2, %3 offen offset:1024 sc1 lds\n\t"
                 "buffer_load_dwordx4 %1, %2, %3 offen offset:2048 sc1 lds\n\t"
                 "buffer_load_dwordx4 %1, %2, %3 offen offset:3072 sc1 lds" :: "s"(ldsaddr), "v"(voff), "s"(rs), "s"(soff) : "memory");
}
DI void stflag(LAS char* p, unsigned v) { waitl0(); *(volatile LAS unsigned*)p = v; CFENCE(); }
constexpr u64 upper_mask(int i) {
    u64 m = 0;
    for (int l = 0; l < 64; ++l) if ((i & 3) + 8 * (i >> 2) + 4 * (l >> 5) > (l & 31)) m |= 1ull << l;
    return m;
}
DI float sel(float lo, float hi, u64 m) { return __builtin_amdgcn_inverse_ballot_w64(m) ? hi : lo; }

#define STAMP(i) do {} while (0)
#define CSTAMP(i) do {} while (0)
#define STAMP_FLUSH() do {} while (0)

DI u32x4 unit_exact(LAS char* lds, int hd, int qb, int nskip, int lane) {
    const int r32 = lane & 31, hh = lane >> 5;
    const bf16x8 qf = *(LAS bf16x8*)(lds + L_Q + hd * 2048 + qb * 1024 + lane * 16);
    LAS char* kp = lds + L_K + hd * 10240 + qb * 1024 + lane * 16;
    const int va0 = (r32 < 16) ? (L_V + hd * 10240 + qb * 1024 + hh * 256 + r32 * 16) : (L_ONES + qb * 256);
    const int vstr = (r32 < 16) ? 1024 : 16;
    const float NINF = -__builtin_inff();
    const int t0 = nskip > qb ? nskip - qb : 0;
    const f32x16 zero = {};
    float m = NINF;
#pragma unroll 1
    for (int t = t0; t < 9; ++t) {
        f32x16 S = MFMA32(*(LAS bf16x8*)(kp + t * 1024), qf, zero);
#pragma unroll
        for (int i = 0; i < 16; ++i) {
            const int key = (i & 3) + 8 * (i >> 2) + 4 * hh;
            const bool ok = t == 0 ? key > r32 : t == 8 ? key <= r32 : true;
            m = fmaxf(m, ok ? S[i] : NINF);
        }
    }
    m = xhalf_max(m);
    f32x16 O = {};
#pragma unroll 1
    for (int t = t0; t < 9; ++t) {
        f32x16 S = MFMA32(*(LAS bf16x8*)(kp + t * 1024), qf, zero);
#pragma unroll
        for (int i = 0; i < 16; ++i) {
            const int key = (i & 3) + 8 * (i >> 2) + 4 * hh;
            const bool ok = t == 0 ? key > r32 : t == 8 ? key <= r32 : true;
            S[i] = ok ? __builtin_amdgcn_exp2f(S[i] - m) : 0.f;
        }
        LAS char* vp = lds + va0 + t * vstr;
        O = MFMA32(*(LAS bf16x8*)(vp), as_bf(pack8(S, 0)), O);
        O = MFMA32(*(LAS bf16x8*)(vp + 512), as_bf(pack8(S, 8)), O);
    }
    const float rl = __builtin_amdgcn_rcpf(O[8]);
#pragma unroll
    for (int i = 0; i < 8; ++i) O[i] *= rl;
    return pack8(O, 0);
}

__global__ __launch_bounds__(512, 2) void swa_block(const u16* __restrict__ x, const u16* __restrict__ wqkv,
                                                    const u16* __restrict__ wo, u16* __restrict__ out, char* ws) {
    extern __shared__ __attribute__((aligned(16))) char smem_[];
    LAS char* lds = (LAS char*)smem_;
    asm volatile("" :: "s"(x), "s"(wqkv), "s"(wo), "s"(out), "s"(ws));
    const int tid = threadIdx.x;
    const int wave = __builtin_amdgcn_readfirstlane(tid >> 6);
    const int lane = tid & 63, r32 = lane & 31, hh = lane >> 5;
    const int bid = blockIdx.x, g = bid & 7, ii = bid >> 3;
    const int hg = (g & 1) * 4 + (ii & 3);
    const int ck = (g >> 1) * 8 + (ii >> 2);
    const int rot = ((ii >> 2) + 2 * (ii & 3)) & 7;
    const int orow = g * 256 + (ii >> 3) * 64, ocol = (ii & 7) * 64;
    const __amdgpu_buffer_rsrc_t wsr = __builtin_amdgcn_make_buffer_rsrc(ws, 0, WS_END, 0x00020000);
    const unsigned lbase = (unsigned)(uintptr_t)lds;
    const int hbase = 2 * ck - 8;
    const int nskip = hbase < 0 ? -hbase : 0;
    const int q4 = (lane & 15) >> 2;
    const int hd = wave & 3;
    const bool isL = wave >= 4;
    const int qb = isL ? 1 : 0;
    const int wo_off = hd < 2 ? 81920 + hd * 16384 : (hd - 2) * 16384;
    unsigned stampv = 0; (void)stampv;
    STAMP(0); CSTAMP(62);
    const unsigned wchunk = (unsigned)(((((lane >> 2) & 1) ^ ((lane >> 4) & 1)) * 4 + (lane & 3)) * 16);
    const unsigned myflag = WS_FLAG + (unsigned)(hg * 32 + ck) * 128 + wave * 4;
    const u32x4 sentv = {SENT, SENT, SENT, SENT};
    unsigned tagv;

    u32x4 mk0, mk1;
    u64 um[16];
    unsigned foff_h, foff_o;
    unsigned hvo, oslot, osrc;
    LAS char *kp, *vp0, *qp, *hchk, *wb, *rpw, *rpr;
    int vstr;
    u16* dst;
    auto prepare = [&]() AINL {
#pragma unroll
        for (int j = 0; j < 4; ++j) {
            const int ka = ((2 * j) & 3) + 8 * ((2 * j) >> 2) + 4 * hh;
            mk0[j] = ((ka > r32) ? 0xFFFFu : 0u) | ((ka + 1 > r32) ? 0xFFFF0000u : 0u);
            mk1[j] = ((ka + 16 > r32) ? 0xFFFFu : 0u) | ((ka + 17 > r32) ? 0xFFFF0000u : 0u);
        }
        PIN(mk0); PIN(mk1);
        const int l8 = lane & 7;
        const int pc = ck - 1 - (l8 >> 1);
        foff_h = WS_FLAG + (unsigned)(hg * 32 + (pc < 0 ? ck : pc)) * 128 + ((hd >> 1) * 2 + (l8 & 1)) * 4;
        foff_o = WS_FLAG + (unsigned)((2 * hd + (l8 >> 2)) * 32 + (orow >> 6)) * 128 + (qb * 4 + (l8 & 3)) * 4;
        hvo = (unsigned)((int)(isL ? WS_K : WS_V) + ((hg * 4 + hd) * 64 + hbase) * 1024 + lane * 16);
        hchk = lds + (isL ? L_K : L_V) + hd * 10240 + lane * 16 + 12;
        osrc = WS_O + (unsigned)(((orow >> 5) + qb) * 32 + hd * 8) * 1024 + lane * 16;
        kp = lds + L_K + hd * 10240 + qb * 1024 + lane * 16;
        qp = lds + L_Q + hd * 2048 + qb * 1024 + lane * 16;
        vp0 = lds + ((r32 < 16) ? (L_V + hd * 10240 + qb * 1024 + hh * 256 + r32 * 16) : (L_ONES + qb * 256));
        vstr = (r32 < 16) ? 1024 : 16;
        wb = lds + wo_off + (4 * hh + q4) * 128 + ((lane >> 4) & 1) * 32 + (lane & 3) * 8 + ((q4 >> 1) << 6);
        rpw = lds + L_RED + wave * 9216 + r32 * 144 + hh * 16;
        const int qr = hd * 8 + (lane >> 3), co = lane & 7;
        rpr = lds + L_RED + (wave & 4) * 9216 + (co >> 2) * 4608 + qr * 144 + (co & 3) * 32;
        dst = out + (size_t)(orow + qb * 32 + qr) * 512 + ocol + co * 8;
        PIN(foff_h); PIN(foff_o); PIN(hvo); PIN(hchk); PIN(osrc); PIN(kp); PIN(qp); PIN(vp0); PIN(vstr); PIN(wb); PIN(rpw); PIN(rpr); PIN(dst);
    };
    oslot = WS_O + (unsigned)((ck * 2 + qb) * 32 + hg * 4 + hd) * 1024 + lane * 16;
    auto make_um = [&]() AINL {
#pragma unroll
        for (int i = 0; i < 16; ++i) { um[i] = upper_mask(i); asm volatile("" : "+s"(um[i])); }
    };
    const unsigned hdst = lbase + (isL ? L_K : L_V) + hd * 10240;
    auto issue_halo = [&]() AINL {
#pragma unroll
        for (int i = 0; i < 8; ++i) blds16_sc1(wsr, hvo, i >= nskip ? i * 1024 : OOB, hdst + i * 1024);
    };
    auto issue_halo_slow = [&]() AINL {
#pragma unroll 1
        for (int i = 0; i < 8; ++i) blds16_sc1(wsr, hvo, __builtin_amdgcn_readfirstlane(i >= nskip ? i * 1024 : OOB), __builtin_amdgcn_readfirstlane(hdst + i * 1024));
    };
    auto halo_probe = [&]() AINL -> bool {
        unsigned mx = 0;
        CFENCE();
#pragma unroll
        for (int t = 0; t < 8; ++t) mx = max(mx, *(LAS unsigned*)(hchk + t * 1024));
        return __all(mx != SENT);
    };
    auto halo_check = [&]() AINL {
        if (UNLIKELY(!halo_probe())) {
            STAMP(28);
#pragma unroll 1
            for (int spin = 0; spin < SPIN_CAP; ++spin) {
                issue_halo_slow();
                waitv<0>();
                if (halo_probe()) break;
            }
        }
    };
    auto flag_load = [&](unsigned off) AINL -> unsigned {
        unsigned f;
        asm volatile("buffer_load_dword %0, %1, %2, 0 offen sc1" : "=v"(f) : "v"(off), "s"(wsr) : "memory");
        return f;
    };
    auto flag_poll = [&](unsigned f, unsigned off) AINL {
        if (UNLIKELY(!__all(f == tagv))) {
#pragma unroll 1
            for (int spin = 0; spin < SPIN_CAP; ++spin) {
                __builtin_amdgcn_s_sleep(1);
                asm volatile("buffer_load_dword %0, %1, %2, 0 offen sc1\n\ts_waitcnt vmcnt(0)" : "=v"(f) : "v"(off), "s"(wsr) : "memory");
                if (__all(f == tagv)) break;
            }
        }
    };
    unsigned ocf = 0;
    u32x4 ov;
    bool ok;

    auto attention = [&](auto ISL) AINL {
        constexpr bool L = decltype(ISL)::value;
        u32x4 P[9][2];
        f32x16 cn, T0, T1, Sb, O;
        const f32x16 zero = {};
        const bf16x8 qf = *(LAS bf16x8*)qp;
        auto kread = [&](int t) AINL { return *(LAS bf16x8*)(kp + t * 1024); };
        auto exps = [&](int t, f32x16& T) AINL {
#pragma unroll
            for (int i = 0; i < 16; ++i) T[i] = __builtin_amdgcn_exp2f(T[i]);
            P[t][0] = pack8(T, 0); P[t][1] = pack8(T, 8);
            PIN(P[t][0]); PIN(P[t][1]);
            SB();
        };
        bf16x8 vf[9][2];
        auto vread = [&](int t) AINL { LAS char* vp = vp0 + t * vstr; vf[t][0] = *(LAS bf16x8*)(vp); vf[t][1] = *(LAS bf16x8*)(vp + 512); };
        auto pvh = [&](int t, int h) AINL { O = MFMA32(vf[t][h], as_bf(P[t][h]), O); };
        const u16* wop = wo + (size_t)(hd * 128) * 512 + ocol;
        const unsigned ovo = (unsigned)((lane >> 3) * 1024) + wchunk;
        auto issue_wo = [&](int i0) AINL {
#pragma unroll
            for (int i = i0; i < i0 + 4; ++i) glds16(wop + (size_t)i * 4096, ovo, lbase + wo_off + i * 1024);
        };
        {
            const bf16x8 k7 = kread(7);
            f32x16 S = MFMA32(k7, qf, zero);
            float m = fmaxf(S[0], S[1]);
#pragma unroll
            for (int i = 2; i < 16; i += 2) m = fmaxf(fmaxf(m, S[i]), S[i + 1]);
            const float nm = -(xhalf_max(m) + HEADROOM);
#pragma unroll
            for (int i = 0; i < 16; ++i) cn[i] = nm;
            T1 = MFMA32(k7, qf, cn);
        }
        if constexpr (L) {
            SB();
            exps(7, T1);
            waitv<1>();
            halo_check();
            stflag(lds + L_FL + hd * 4, 2);
            STAMP(11);
            T0 = MFMA32(kread(6), qf, cn);
        } else {
            T0 = MFMA32(kread(6), qf, cn);
            SB();
            exps(7, T1);
            issue_halo();
            STAMP(12);
        }
#pragma unroll
        for (int t = 6; t >= 2; --t) {
            f32x16& Tc = (t & 1) ? T1 : T0;
            f32x16& Tn = (t & 1) ? T0 : T1;
            Tn = MFMA32(kread(t - 1), qf, cn);
            SB();
            exps(t, Tc);
            if constexpr (L) {
                if (t == 6) {
                    waitv<0>();
                    if (lane == 0) __builtin_amdgcn_raw_buffer_store_b32(tagv, wsr, myflag, 0, 16);
                }
                if (t == 5) wait4_ge(lds + L_FG, 1);
                if (t <= 5) issue_wo((5 - t) * 4);
            }
            if (t == 5) STAMP(21);
        }
        T0 = MFMA32(kread(0), qf, cn);
        Sb = MFMA32(kread(8), qf, cn);
        STAMP(13);
        if constexpr (L) {
            wait1_ge(lds + L_FG + hd * 4, 2);
        } else {
            waitv<0>();
            halo_check();
            stflag(lds + L_FG + hd * 4, 2);
            wait4_ge(lds + L_FG, 1);
        }
        STAMP(14);
        ocf = flag_load(foff_o);
        vread(7); vread(6); vread(5);
        SB();
        O = zero;
#pragma unroll
        for (int c = 0; c < 4; ++c) {
#pragma unroll
            for (int i = 4 * c; i < 4 * c + 4; ++i) T0[i] = sel(Sb[i], T0[i], um[i]);
            pvh(7 - c, 0);
            if (c < 3) vread(4 - c);
            SB();
#pragma unroll
            for (int i = 4 * c; i < 4 * c + 4; ++i) T0[i] = __builtin_amdgcn_exp2f(T0[i]);
            pvh(7 - c, 1);
            SB();
        }
        {
            const u32x4 p0 = pack8(T0, 0);
            pvh(3, 0);
            vread(0);
            SB();
            const u32x4 p1 = pack8(T0, 8);
            P[0][0] = p0 & mk0; P[8][0] = p0 ^ P[0][0];
            pvh(3, 1);
            vread(8);
            SB();
            P[0][1] = p1 & mk1; P[8][1] = p1 ^ P[0][1];
            pvh(2, 0);
            vread(1);
            SB();
        }
        {
#pragma unroll
            for (int c = 0; c < 4; ++c) {
#pragma unroll
                for (int i = 4 * c; i < 4 * c + 4; ++i) T1[i] = __builtin_amdgcn_exp2f(T1[i]);
                if (c == 0) pvh(2, 1);
                if (c == 1) pvh(0, 0);
                if (c == 2) pvh(0, 1);
                if (c == 3) pvh(8, 0);
                SB();
            }
            P[1][0] = pack8(T1, 0);
            pvh(8, 1);
            SB();
            P[1][1] = pack8(T1, 8);
            pvh(1, 0); pvh(1, 1);
        }
        const float l = O[8];
        const float rl = __builtin_amdgcn_rcpf(l);
        float chk = 0.f;
#pragma unroll
        for (int i = 0; i < 8; ++i) { O[i] *= rl; chk += O[i]; }
        ov = pack8(O, 0);
        ok = __all(l < 0x1p126f && l > 0x1p-100f && __builtin_fabsf(chk) < __builtin_inff());
        if constexpr (L) { waitv<1>(); stflag(lds + L_FL + hd * 4, 3); }
    };

    if (isL) {
        const int lw = hd;
        const unsigned xvo = (unsigned)((lane >> 3) * 512 + (((lane & 7) ^ (lane >> 4) ^ ((lw & 1) << 2)) * 8)) * 2u;
        const unsigned wvo = (unsigned)((lane >> 3) * 3072) + wchunk;
        const u16* xsrc = x + (size_t)(ck * 64 + lw * 8) * 512;
        const u16* wsrc = wqkv + (size_t)(lw * 8) * 1536 + hg * 64;
        auto issue_w = [&](int s, int col, unsigned d) AINL {
            const u16* wp = wsrc + (size_t)((s + rot) & 7) * 64 * 1536 + col;
            glds16(wp, wvo, d);
            glds16(wp + 32 * 1536, wvo, d + 4096);
        };
        auto issue_step = [&](int n) AINL {
            const unsigned d = lbase + L_RING + slot_of(n) + lw * 1024;
            if (n < 8) {
                const u16* xp = xsrc + ((n + rot) & 7) * 64;
                glds16(xp, xvo, d);
                glds16(xp + 32 * 512, xvo, d + 4096);
                issue_w(n, 512, d + 8192);
            } else {
                issue_w(2 * (n & 3), n < 12 ? 0 : 1024, d);
                issue_w(2 * (n & 3) + 1, n < 12 ? 0 : 1024, d + 8192);
            }
        };
        issue_step(0); issue_step(1); issue_step(2); issue_step(3);
        unsigned tag, kfl = 0;
        asm volatile("s_load_dword %0, %1, 0x0" : "=s"(tag) : "s"(ws) : "memory");
        STAMP(18);
        prepare();
#pragma unroll
        for (int n = 0; n < 12; ++n) {
            if (GAP8 && n == 9) waitv<8>(); else if (n == 11) waitv<13>(); else waitv<12>();
            __builtin_amdgcn_s_barrier();
            if (n == 0) STAMP(20);
            if (n == 10) kfl = flag_load(foff_h);
            if (GAP8 && n == 9) issue_step(12);
            if (!GAP8 || n != 8) issue_step(n + 4);
        }
        make_um();
        asm volatile("s_waitcnt vmcnt(8) lgkmcnt(0)" : "+s"(tag), "+v"(kfl) :: "memory");
        tagv = tag + 1u;
        flag_poll(kfl, foff_h);
        issue_halo();
        __builtin_amdgcn_raw_buffer_store_b128(sentv, wsr, oslot, 0, 16);
        STAMP(16);
        __builtin_amdgcn_s_barrier();
        STAMP(5);
        waitv<9>();
        stflag(lds + L_FL + lw * 4, 1);
        STAMP(17);
        attention(IC<1>{});
    } else {
        const int ft = wave >> 1, tb = wave & 1;
        asm volatile("buffer_load_dword %0, off, %1, 0 sc1" : "=v"(tagv) : "s"(wsr) : "memory");
        const unsigned kb0 = (unsigned)((hg * 4 + 2 * ft) * 64 + ck * 2 + tb) * 1024;
        unsigned kdst = WS_K + kb0 + lane * 16;
        const int vsub = hh * 256 + (r32 & 15) * 16;
        unsigned vdst = WS_V + kb0 + (r32 >> 4) * 65536 + vsub;
        auto clear = [&](int i) AINL {
            if (i == 0) __builtin_amdgcn_raw_buffer_store_b128(sentv, wsr, kdst, 0, 16);
            if (i == 1) __builtin_amdgcn_raw_buffer_store_b128(sentv, wsr, kdst, 65536, 16);
            if (i == 2) __builtin_amdgcn_raw_buffer_store_b128(sentv, wsr, vdst, 0, 16);
            if (i == 3) __builtin_amdgcn_raw_buffer_store_b128(sentv, wsr, vdst + 512, 0, 16);
            if (i == 4) __builtin_amdgcn_raw_buffer_store_b128(sentv, wsr, oslot, 0, 16);
        };
        {
            const unsigned o1 = (lane & 15) + ((lane >> 4) & 1) >= nskip ? 0x3F803F80u : 0u;
            const u32x4 one = {o1, o1, o1, o1}, zz = {};
            *(LAS u32x4*)(lds + L_ONES + lane * 16) = one;
            *(LAS u32x4*)(lds + L_FL + (lane & 1) * 16) = zz;
        }
        prepare();
        LAS char* xa[4];
        {
            const int u = hh ^ ((r32 >> 1) & 7);
#pragma unroll
            for (int ks = 0; ks < 4; ++ks) { xa[ks] = lds + L_RING + (tb * 32 + r32) * 128 + (((2 * ks) ^ u) << 4); PIN(xa[ks]); }
        }
        LAS char* wa0 = lds + (8 * hh + q4) * 128 + ((ft ^ (q4 >> 1)) << 6) + ((lane >> 4) & 1) * 32 + (lane & 3) * 8;
        LAS char* wa1 = wa0 + 65536;
        LAS char* vl = lds + L_V + (2 * ft + (r32 >> 4)) * 10240 + (8 + tb) * 1024 + vsub;
        LAS char* kl = lds + L_K + (2 * ft) * 10240 + (8 + tb) * 1024 + lane * 16;
        LAS char* ql = lds + L_Q + (2 * ft) * 2048 + tb * 1024 + lane * 16;
        PIN(wa0); PIN(wa1); PIN(vdst); PIN(kdst); PIN(vl); PIN(kl); PIN(ql);
        auto wrd = [&](int off) AINL { LAS char* p = off < 65536 ? wa0 + off : wa1 + (off - 65536); return tr2(p, p + 512); };
        bf16x8 xf[32], wf[2][8];
        f32x16 acc = {};
#pragma unroll
        for (int n = 0; n <= 12; ++n) {
            const int slot = slot_of(n);
            const int p = n - 1;
            if (n < 12) {
                if (n == 0) waitl0();
                __builtin_amdgcn_s_barrier();
                CFENCE();
                STAMP(30 + n);
            }
            if (p == 8) acc = f32x16{};
            SB();
#pragma unroll
            for (int j = 0; j < 8; ++j) {
                if (p >= 0 && p < 8 && j < 4) acc = MFMA32(wf[p & 1][j], xf[4 * p + j], acc);
                if (p >= 8) acc = MFMA32(wf[p & 1][j], xf[8 * (p - 8) + j], acc);
                if (n < 8 && j < 4) {
                    xf[4 * n + j] = *(LAS bf16x8*)(xa[j] + slot);
                    wf[n & 1][j] = wrd(slot + 8192 + j * 2048);
                }
                if (n == 8 && j < 4) {
                    wf[n & 1][2 * j] = wrd(slot + j * 4096);
                    wf[n & 1][2 * j + 1] = wrd(slot + j * 4096 + 2048);
                }
                if (n > 8 && n < 12) wf[n & 1][j] = wrd(slot + j * 2048);
                SB();
            }
            if (n == 0) { clear(0); clear(1); clear(2); clear(3); clear(4); }
            if (p == 7) {
                const u32x4 k0 = sanitize(pack8(acc, 0)), k1 = sanitize(pack8(acc, 8));
                __builtin_amdgcn_raw_buffer_store_b128(k0, wsr, kdst, 0, 16);
                __builtin_amdgcn_raw_buffer_store_b128(k1, wsr, kdst, 65536, 16);
                *(LAS u32x4*)(kl) = k0;
                *(LAS u32x4*)(kl + 10240) = k1;
                STAMP(2);
            }
            if (n == AFLAG_POS) {
                asm volatile("s_waitcnt vmcnt(0)" : "+v"(tagv) :: "memory");
                tagv += 1u;
                __builtin_amdgcn_raw_buffer_store_b32(tagv, wsr, myflag, 0, 16);
                STAMP(26);
            }
        }
#pragma unroll
        for (int i = 0; i < 16; ++i) acc[i] *= CEXP;
        *(LAS u32x4*)(ql) = pack8(acc, 0);
        *(LAS u32x4*)(ql + 2048) = pack8(acc, 8);
        STAMP(4);
        bar();
        STAMP(5);
        __builtin_amdgcn_s_setprio(2);
        acc = f32x16{};
#pragma unroll
        for (int j = 0; j < 8; ++j) wf[0][j] = wrd(slot_of(12) + j * 2048);
        make_um();
        SB();
#pragma unroll
        for (int pr = 0; pr < 4; ++pr) {
            if (pr == 1) wait4_ge(lds + L_FL, 1);
#pragma unroll
            for (int j = 0; j < 8; ++j) {
                acc = MFMA32(xf[8 * pr + j], wf[pr & 1][j], acc);
                if (pr < 3) wf[(pr + 1) & 1][j] = wrd(slot_of(13 + pr) + j * 2048);
                SB();
            }
        }
        {
            const u32x4 v0 = sanitize(pack8(acc, 0)), v1 = sanitize(pack8(acc, 8));
            __builtin_amdgcn_raw_buffer_store_b128(v0, wsr, vdst, 0, 16);
            __builtin_amdgcn_raw_buffer_store_b128(v1, wsr, vdst + 512, 0, 16);
            *(LAS u32x4*)(vl) = v0;
            *(LAS u32x4*)(vl + 512) = v1;
            stflag(lds + L_FG + wave * 4, 1);
        }
        __builtin_amdgcn_s_setprio(0);
        STAMP(3);
        wait1_ge(lds + L_FL + hd * 4, 2);
        STAMP(27);
        attention(IC<0>{});
    }

    STAMP(25);
    if (UNLIKELY(!ok)) { STAMP(15); ov = unit_exact(lds, hd, qb, nskip, lane); }
    __builtin_amdgcn_raw_buffer_store_b128(sanitize(ov), wsr, oslot, 0, 16);
    STAMP(6);

    {
        asm volatile("s_waitcnt vmcnt(1)" : "+v"(ocf) :: "memory");
        flag_poll(ocf, foff_o);
        wait1_ge(lds + L_FL + hd * 4, 3);
        bf16x8 wa[8][2];
        {
            LAS char* wb2 = lds + (int)((unsigned)(wb - lds) ^ 64u);
#pragma unroll
            for (int j = 0; j < 8; ++j) {
                wa[j][0] = tr2(wb + j * 2048, wb + j * 2048 + 1024);
                wa[j][1] = tr2(wb2 + j * 2048, wb2 + j * 2048 + 1024);
            }
        }
        u32x4 d[8];
        {
            const unsigned pofs = osrc - lane * 16 + (lane & 7) * 1024 + 1020;
            unsigned got = 0;
#pragma unroll 1
            for (int spin = 0; spin < SPIN_CAP; ++spin) {
                CFENCE();
                const unsigned pr = __builtin_amdgcn_raw_buffer_load_b32(wsr, pofs, 0, 16);
                const unsigned rdy = (unsigned)__builtin_amdgcn_ballot_w64(pr != SENT) & 0xFFu;
                const unsigned nw = rdy & ~got;
#pragma unroll
                for (int j = 0; j < 8; ++j)
                    if (nw >> j & 1) d[j] = __builtin_amdgcn_raw_buffer_load_b128(wsr, osrc + j * 1024, 0, 16);
                got |= rdy;
                if (got == 0xFFu) break;
                if (spin == 0) STAMP(7);
                __builtin_amdgcn_s_sleep(PROBE_SLEEP);
            }
        }
        f32x16 c0 = {}, c1 = {};
        unsigned mx = 0;
#pragma unroll
        for (int j = 0; j < 8; ++j) {
            c0 = MFMA32(wa[j][0], as_bf(d[j]), c0);
            c1 = MFMA32(wa[j][1], as_bf(d[j]), c1);
            mx = max(mx, d[j].w);
        }
        if (UNLIKELY(!__all(mx != SENT))) {
#pragma unroll 1
            for (int spin = 0; spin < SPIN_CAP; ++spin) {
                bool pend = false;
                CFENCE();
#pragma unroll
                for (int j = 0; j < 8; ++j)
                    if (!__all(d[j].w != SENT)) { d[j] = __builtin_amdgcn_raw_buffer_load_b128(wsr, osrc + j * 1024, 0, 16); pend = true; }
                if (!pend) break;
            }
            c0 = f32x16{}; c1 = f32x16{};
#pragma unroll
            for (int j = 0; j < 8; ++j) {
                c0 = MFMA32(wa[j][0], as_bf(d[j]), c0);
                c1 = MFMA32(wa[j][1], as_bf(d[j]), c1);
            }
        }
        STAMP(8);
        if (UNLIKELY(bid == 0 && wave == 7)) {
#pragma unroll 1
            for (int spin = 0; spin < SPIN_CAP; ++spin) {
                CFENCE();
                bool all = true;
#pragma unroll
                for (int i = 0; i < 8; ++i) {
                    const u32x4 f = __builtin_amdgcn_raw_buffer_load_b128(wsr, WS_FLAG + ((i >> 1) * 64 + lane) * 128 + (i & 1) * 16, 0, 16);
                    all &= f.x == tagv && f.y == tagv && f.z == tagv && f.w == tagv;
                }
                if (__all(all)) break;
            }
            if (lane == 0) __builtin_amdgcn_raw_buffer_store_b32(tagv, wsr, WS_EPOCH, 0, 16);
        }
        bar();
#pragma unroll
        for (int gq = 0; gq < 4; ++gq) {
            f32x4 a = {c0[4 * gq], c0[4 * gq + 1], c0[4 * gq + 2], c0[4 * gq + 3]};
            f32x4 b = {c1[4 * gq], c1[4 * gq + 1], c1[4 * gq + 2], c1[4 * gq + 3]};
            *(LAS f32x4*)(rpw + gq * 32) = a;
            *(LAS f32x4*)(rpw + 4608 + gq * 32) = b;
        }
        bar();
        STAMP(9);
        {
            f32x4 a = *(LAS f32x4*)(rpr), b = *(LAS f32x4*)(rpr + 16);
#pragma unroll
            for (int s = 1; s < 4; ++s) { a += *(LAS f32x4*)(rpr + s * 9216); b += *(LAS f32x4*)(rpr + s * 9216 + 16); }
            const u32x4 r = {pack2(a[0], a[1]), pack2(a[2], a[3]), pack2(b[0], b[1]), pack2(b[2], b[3])};
            *(u32x4*)dst = r;
        }
        STAMP(10); CSTAMP(63); STAMP_FLUSH();
    }
}

extern "C" void kernel_launch(void* const* d_in, const int* in_sizes, int n_in, void* d_out, int out_size, void* d_ws,
                              size_t ws_size, hipStream_t stream) {
    (void)in_sizes; (void)n_in; (void)out_size; (void)ws_size;
    (void)hipFuncSetAttribute((const void*)swa_block, hipFuncAttributeMaxDynamicSharedMemorySize, LDS_BYTES);
    swa_block<<<256, 512, LDS_BYTES, stream>>>((const u16*)d_in[0], (const u16*)d_in[1], (const u16*)d_in[2], (u16*)d_out, (char*)d_ws);
}
```

```cpp
#include <hip/hip_runtime.h>
#include <stdint.h>
#include <type_traits>

typedef short bf16x8 __attribute__((ext_vector_type(8)));
typedef short s16x4 __attribute__((ext_vector_type(4)));
typedef float f32x16 __attribute__((ext_vector_type(16)));
typedef float f32x4 __attribute__((ext_vector_type(4)));
typedef float f32x2 __attribute__((ext_vector_type(2)));
typedef unsigned u32x4 __attribute__((ext_vector_type(4)));
typedef unsigned u32x2 __attribute__((ext_vector_type(2)));
typedef __bf16 bf16x2 __attribute__((ext_vector_type(2)));
typedef unsigned short u16;
typedef unsigned long long u64;
#define LAS __attribute__((address_space(3)))
#define DI __device__ __forceinline__

#define AFLAG_POS 6
#define PROBE_SLEEP 2
#define DIRECT_TAIL 4
#define LOOK 3
#define WO_G 1
#define KH_AT 13

constexpr unsigned WS_EPOCH = 0;
constexpr unsigned WS_FLAG = 1024;
constexpr unsigned WS_K = 65536;
constexpr unsigned WS_V = WS_K + 32 * 64 * 1024;
constexpr unsigned WS_O = WS_V + 32 * 64 * 1024;
constexpr unsigned WS_END = WS_O + 64 * 32 * 1024;
constexpr int L_RING = 0;
constexpr int L_K = 65536;
constexpr int L_V = L_K + 40960;
constexpr int L_Q = L_V + 40960;
constexpr int L_ONES = L_Q + 8192;
constexpr int L_RED = L_K;
constexpr int LDS_BYTES = L_ONES + 1024;
constexpr int slot_of(int n) { return (n & 3) * 16384; }

constexpr float CEXP = 0.36067376022224085f;
constexpr float HEADROOM = 32.f;
constexpr unsigned SENT = 0xFFFFFFFFu;
constexpr unsigned OOB = 0x40000000u;
constexpr int SPIN_CAP = 1 << 18;

DI unsigned pack2(float a, float b) {
    f32x2 v = {a, b};
    return __builtin_bit_cast(unsigned, __builtin_convertvector(v, bf16x2));
}
DI u32x4 pack8(const f32x16& a, int o) {
    u32x4 r = {pack2(a[o], a[o + 1]), pack2(a[o + 2], a[o + 3]), pack2(a[o + 4], a[o + 5]), pack2(a[o + 6], a[o + 7])};
    return r;
}
DI u32x4 sanitize(u32x4 v) {
    v.w = min(v.w, SENT - 1);
    return v;
}
DI bf16x8 as_bf(u32x4 v) { return __builtin_bit_cast(bf16x8, v); }
DI u32x4 as_u(bf16x8 v) { return __builtin_bit_cast(u32x4, v); }
DI void waitl0() { asm volatile("" ::: "memory"); __builtin_amdgcn_s_waitcnt(0xC07F); asm volatile("" ::: "memory"); }
DI void bar() { waitl0(); __builtin_amdgcn_s_barrier(); asm volatile("" ::: "memory"); }
template <int N> DI void waitv() { asm volatile("s_waitcnt vmcnt(%0)" :: "i"(N) : "memory"); }
#define CFENCE() asm volatile("" ::: "memory")
#define SB() __builtin_amdgcn_sched_barrier(0)
#define PIN(x) asm volatile("" : "+v"(x))
#define MFMA32(a, b, c) __builtin_amdgcn_mfma_f32_32x32x16_bf16((a), (b), (c), 0, 0, 0)
DI s16x4 trread(LAS char* p) { return __builtin_amdgcn_ds_read_tr16_b64_v4i16((LAS s16x4*)p); }
DI bf16x8 tr2(LAS char* p0, LAS char* p1) {
    s16x4 a = trread(p0), b = trread(p1);
    return __builtin_shufflevector(a, b, 0, 1, 2, 3, 4, 5, 6, 7);
}
DI void glds16(const void* sbase, unsigned voff, unsigned ldsaddr) {
    asm volatile("s_mov_b32 m0, %0\n\ts_nop 0\n\tglobal_load_lds_dwordx4 %1, %2" :: "s"(ldsaddr), "v"(voff), "s"(sbase) : "memory");
}
DI void blds16_sc1(__amdgpu_buffer_rsrc_t rs, unsigned voff, unsigned soff, unsigned ldsaddr) {
    asm volatile("s_mov_b32 m0, %0\n\ts_nop 0\n\tbuffer_load_dwordx4 %1, %2, %3 offen sc1 lds" :: "s"(ldsaddr), "v"(voff), "s"(rs), "s"(soff) : "memory");
}
DI float xhalf_max(float m) {
    auto r = __builtin_amdgcn_permlane32_swap(__float_as_uint(m), __float_as_uint(m), false, false);
    return fmaxf(__uint_as_float(r[0]), __uint_as_float(r[1]));
}
DI unsigned min4(u32x4 f) { return __builtin_amdgcn_readfirstlane(min(min(f.x, f.y), min(f.z, f.w))); }
DI u32x4 ldflag4(LAS char* p) { CFENCE(); u32x4 f = *(volatile LAS u32x4*)p; CFENCE(); return f; }
DI unsigned ldflag1(LAS char* p) { CFENCE(); unsigned f = *(volatile LAS unsigned*)p; CFENCE(); return __builtin_amdgcn_readfirstlane(f); }
#define UNLIKELY(c) __builtin_expect(!!(c), 0)
#define AINL __attribute__((always_inline))
template <int V> using IC = std::integral_constant<int, V>;
DI void wait4_ge(LAS char* p, unsigned n) {
    if (UNLIKELY(min4(ldflag4(p)) < n)) {
#pragma unroll 1
        for (int spin = 0; spin < SPIN_CAP; ++spin) if (min4(ldflag4(p)) >= n) break;
    }
}
DI void wait1_ge(LAS char* p, unsigned n) {
    if (UNLIKELY(ldflag1(p) < n)) {
#pragma unroll 1
        for (int spin = 0; spin < SPIN_CAP; ++spin) if (ldflag1(p) >= n) break;
    }
}
DI void blds4k_sc1(__amdgpu_buffer_rsrc_t rs, unsigned voff, unsigned soff, unsigned ldsaddr) {
    asm volatile("s_mov_b32 m0, %0\n\ts_nop 0\n\tbuffer_load_dwordx4 %1, %2, %3 offen sc1 lds\n\t"
                 "buffer_load_dwordx4 %1, %2, %3 offen offset:1024 sc1 lds\n\t"
                 "buffer_load_dwordx4 %1, %2, %3 offen offset:2048 sc1 lds\n\t"
                 "buffer_load_dwordx4 %1, %2, %3 offen offset:3072 sc1 lds" :: "s"(ldsaddr), "v"(voff), "s"(rs), "s"(soff) : "memory");
}
DI void stflag(LAS char* p, unsigned v) { waitl0(); *(volatile LAS unsigned*)p = v; CFENCE(); }
constexpr u64 upper_mask(int i) {
    u64 m = 0;
    for (int l = 0; l < 64; ++l) if ((i & 3) + 8 * (i >> 2) + 4 * (l >> 5) > (l & 31)) m |= 1ull << l;
    return m;
}
DI float sel(float lo, float hi, u64 m) { return __builtin_amdgcn_inverse_ballot_w64(m) ? hi : lo; }

#define STAMP(i) do {} while (0)
#define CSTAMP(i) do {} while (0)
#define STAMP_FLUSH() do {} while (0)

DI u32x4 unit_exact(LAS char* lds, int hd, int qb, int nskip, int lane) {
    const int r32 = lane & 31, hh = lane >> 5;
    const bf16x8 qf = *(LAS bf16x8*)(lds + L_Q + hd * 2048 + qb * 1024 + lane * 16);
    LAS char* kp = lds + L_K + hd * 10240 + qb * 1024 + lane * 16;
    const int va0 = (r32 < 16) ? (L_V + hd * 10240 + qb * 1024 + hh * 256 + r32 * 16) : (L_ONES + qb * 256);
    const int vstr = (r32 < 16) ? 1024 : 16;
    const float NINF = -__builtin_inff();
    const int t0 = nskip > qb ? nskip - qb : 0;
    const f32x16 zero = {};
    float m = NINF;
#pragma unroll 1
    for (int t = t0; t < 9; ++t) {
        f32x16 S = MFMA32(*(LAS bf16x8*)(kp + t * 1024), qf, zero);
#pragma unroll
        for (int i = 0; i < 16; ++i) {
            const int key = (i & 3) + 8 * (i >> 2) + 4 * hh;
            const bool ok = t == 0 ? key > r32 : t == 8 ? key <= r32 : true;
            m = fmaxf(m, ok ? S[i] : NINF);
        }
    }
    m = xhalf_max(m);
    f32x16 O = {};
#pragma unroll 1
    for (int t = t0; t < 9; ++t) {
        f32x16 S = MFMA32(*(LAS bf16x8*)(kp + t * 1024), qf, zero);
#pragma unroll
        for (int i = 0; i < 16; ++i) {
            const int key = (i & 3) + 8 * (i >> 2) + 4 * hh;
            const bool ok = t == 0 ? key > r32 : t == 8 ? key <= r32 : true;
            S[i] = ok ? __builtin_amdgcn_exp2f(S[i] - m) : 0.f;
        }
        LAS char* vp = lds + va0 + t * vstr;
        O = MFMA32(*(LAS bf16x8*)(vp), as_bf(pack8(S, 0)), O);
        O = MFMA32(*(LAS bf16x8*)(vp + 512), as_bf(pack8(S, 8)), O);
    }
    const float rl = __builtin_amdgcn_rcpf(O[8]);
#pragma unroll
    for (int i = 0; i < 8; ++i) O[i] *= rl;
    return pack8(O, 0);
}

__global__ __launch_bounds__(512, 2) void swa_block(const u16* __restrict__ x, const u16* __restrict__ wqkv,
                                                    const u16* __restrict__ wo, u16* __restrict__ out, char* ws) {
    extern __shared__ __attribute__((aligned(16))) char smem_[];
    LAS char* lds = (LAS char*)smem_;
    asm volatile("" :: "s"(x), "s"(wqkv), "s"(wo), "s"(out), "s"(ws));
    const int tid = threadIdx.x;
    const int wave = __builtin_amdgcn_readfirstlane(tid >> 6);
    const int lane = tid & 63, r32 = lane & 31, hh = lane >> 5;
    const int bid = blockIdx.x, g = bid & 7, ii = bid >> 3;
    const int hg = (g & 1) * 4 + (ii & 3);
    const int ck = (g >> 1) * 8 + (ii >> 2);
    const int rot = ((ii >> 2) + 2 * (ii & 3)) & 7;
    const unsigned lbase = (unsigned)(uintptr_t)lds;
    const int hd = wave & 3;
    const bool isL = wave >= 4;
    unsigned stampv = 0; (void)stampv;
    STAMP(0); CSTAMP(62);
    const unsigned wchunk = (unsigned)(((((lane >> 2) & 1) ^ ((lane >> 4) & 1)) * 4 + (lane & 3)) * 16);
    const unsigned xvo = (unsigned)((lane >> 3) * 512 + (((lane & 7) ^ (lane >> 4) ^ ((hd & 1) << 2)) * 8)) * 2u;
    const unsigned wvo = (unsigned)((lane >> 3) * 3072) + wchunk;
    const u16* xsrc = x + (size_t)(ck * 64 + hd * 8) * 512;
    const u16* wsrc = wqkv + (size_t)(hd * 8) * 1536 + hg * 64;
    auto issue_w = [&](int s, int col, unsigned d) AINL {
        const u16* wp = wsrc + (size_t)((s + rot) & 7) * 64 * 1536 + col;
        glds16(wp, wvo, d);
        glds16(wp + 32 * 1536, wvo, d + 4096);
    };
    auto issue_step = [&](int n) AINL {
        const unsigned d = lbase + L_RING + slot_of(n) + hd * 1024;
        if (n < 8) {
            const u16* xp = xsrc + ((n + rot) & 7) * 64;
            glds16(xp, xvo, d);
            glds16(xp + 32 * 512, xvo, d + 4096);
            issue_w(n, 512, d + 8192);
        } else {
            issue_w(2 * (n & 3), n < 12 ? 1024 : 0, d);
            issue_w(2 * (n & 3) + 1, n < 12 ? 1024 : 0, d + 8192);
        }
    };
    if (__builtin_expect(isL, 1)) { issue_step(0); issue_step(1); issue_step(2); if (LOOK == 3) issue_step(3); }
    const int orow = g * 256 + (ii >> 3) * 64, ocol = (ii & 7) * 64;
    const __amdgpu_buffer_rsrc_t wsr = __builtin_amdgcn_make_buffer_rsrc(ws, 0, WS_END, 0x00020000);
    const int hbase = 2 * ck - 8;
    const int nskip = hbase < 0 ? -hbase : 0;
    const int q4 = (lane & 15) >> 2;
    const int qb = isL ? 1 : 0;
    const unsigned myflag = WS_FLAG + (unsigned)(hg * 32 + ck) * 128 + wave * 4;
    const u32x4 sentv = {SENT, SENT, SENT, SENT};
    unsigned tagv;

    u32x4 mk0, mk1;
    u64 um[16];
    unsigned foff_h, foff_o;
    unsigned hvo, oslot, osrc;
    LAS char *kp, *vp0, *qp, *hchk, *wb, *rpw, *rpr;
    int vstr;
    u16* dst;
    auto prepare = [&]() AINL {
#pragma unroll
        for (int j = 0; j < 4; ++j) {
            const int ka = ((2 * j) & 3) + 8 * ((2 * j) >> 2) + 4 * hh;
            mk0[j] = ((ka > r32) ? 0xFFFFu : 0u) | ((ka + 1 > r32) ? 0xFFFF0000u : 0u);
            mk1[j] = ((ka + 16 > r32) ? 0xFFFFu : 0u) | ((ka + 17 > r32) ? 0xFFFF0000u : 0u);
        }
        PIN(mk0); PIN(mk1);
        const int l8 = lane & 7;
        const int pc = ck - 1 - (l8 >> 1);
        foff_h = WS_FLAG + (unsigned)(hg * 32 + (pc < 0 ? ck : pc)) * 128 + ((hd >> 1) * 2 + (l8 & 1)) * 4;
        foff_o = WS_FLAG + (unsigned)((2 * hd + (l8 >> 2)) * 32 + (orow >> 6)) * 128 + (qb * 4 + (l8 & 3)) * 4;
        hvo = (unsigned)((int)WS_K + ((hg * 4 + hd) * 64 + hbase) * 1024 + lane * 16);
        hchk = lds + L_K + hd * 10240 + lane * 16 + 12;
        osrc = WS_O + (unsigned)(((orow >> 5) + qb) * 32 + hd * 8) * 1024 + lane * 16;
        kp = lds + L_K + hd * 10240 + qb * 1024 + lane * 16;
        qp = lds + L_Q + hd * 2048 + qb * 1024 + lane * 16;
        vp0 = lds + ((r32 < 16) ? (L_V + hd * 10240 + qb * 1024 + hh * 256 + r32 * 16) : (L_ONES + qb * 256));
        vstr = (r32 < 16) ? 1024 : 16;
        wb = lds + hd * 16384 + (4 * hh + q4) * 128 + ((lane >> 4) & 1) * 32 + (lane & 3) * 8 + ((q4 >> 1) << 6);
        rpw = lds + L_RED + wave * 9216 + r32 * 144 + hh * 16;
        const int qr = hd * 8 + (lane >> 3), co = lane & 7;
        rpr = lds + L_RED + (wave & 4) * 9216 + (co >> 2) * 4608 + qr * 144 + (co & 3) * 32;
        dst = out + (size_t)(orow + qb * 32 + qr) * 512 + ocol + co * 8;
        PIN(foff_h); PIN(foff_o); PIN(hvo); PIN(hchk); PIN(osrc); PIN(kp); PIN(qp); PIN(vp0); PIN(vstr); PIN(wb); PIN(rpw); PIN(rpr); PIN(dst);
    };
    oslot = WS_O + (unsigned)((ck * 2 + qb) * 32 + hg * 4 + hd) * 1024 + lane * 16;
    auto make_um = [&]() AINL {
#pragma unroll
        for (int i = 0; i < 16; ++i) { um[i] = upper_mask(i); asm volatile("" : "+s"(um[i])); }
    };
    const unsigned hdst = lbase + L_K + hd * 10240;
    auto issue_halo = [&](int v) AINL {
#pragma unroll
        for (int i = 0; i < 8; ++i) blds16_sc1(wsr, hvo, i >= nskip ? i * 1024 + v * (WS_V - WS_K) : OOB, hdst + v * 40960 + i * 1024);
    };
    auto halo_probe = [&](int v) AINL -> bool {
        unsigned mx = 0;
        CFENCE();
#pragma unroll
        for (int t = 0; t < 8; ++t) mx = max(mx, *(LAS unsigned*)(hchk + v * 40960 + t * 1024));
        return __all(mx != SENT);
    };
    auto halo_check = [&](int v) AINL {
        if (UNLIKELY(!halo_probe(v))) {
            STAMP(28);
#pragma unroll 1
            for (int spin = 0; spin < SPIN_CAP; ++spin) {
#pragma unroll 1
                for (int i = 0; i < 8; ++i)
                    blds16_sc1(wsr, hvo, __builtin_amdgcn_readfirstlane(i >= nskip ? i * 1024 + v * (WS_V - WS_K) : OOB), __builtin_amdgcn_readfirstlane(hdst + v * 40960 + i * 1024));
                waitv<0>();
                if (halo_probe(v)) break;
            }
        }
    };
    auto flag_load = [&](unsigned off) AINL -> unsigned {
        unsigned f;
        asm volatile("buffer_load_dword %0, %1, %2, 0 offen sc1" : "=v"(f) : "v"(off), "s"(wsr) : "memory");
        return f;
    };
    auto flag_poll = [&](unsigned f, unsigned off) AINL {
        if (UNLIKELY(!__all(f == tagv))) {
#pragma unroll 1
            for (int spin = 0; spin < SPIN_CAP; ++spin) {
                __builtin_amdgcn_s_sleep(1);
                asm volatile("buffer_load_dword %0, %1, %2, 0 offen sc1\n\ts_waitcnt vmcnt(0)" : "=v"(f) : "v"(off), "s"(wsr) : "memory");
                if (__all(f == tagv)) break;
            }
        }
    };
    unsigned ocf = 0;
    u32x4 ov;
    bool ok;

    auto attention = [&](auto ISL) AINL {
        constexpr bool L = decltype(ISL)::value;
        u32x4 P[9][2];
        f32x16 cn, T0, T1, Sb, O;
        const f32x16 zero = {};
        const bf16x8 qf = *(LAS bf16x8*)qp;
        auto kread = [&](int t) AINL { return *(LAS bf16x8*)(kp + t * 1024); };
        bf16x8 vf[9][2];
        auto vread = [&](int t) AINL { LAS char* vp = vp0 + t * vstr; vf[t][0] = *(LAS bf16x8*)(vp); vf[t][1] = *(LAS bf16x8*)(vp + 512); };
        auto pvh = [&](int t, int h) AINL { O = MFMA32(vf[t][h], as_bf(P[t][h]), O); };
        const u16* wop = wo + (size_t)(hd * 128) * 512 + ocol;
        const unsigned ovo = (unsigned)((lane >> 3) * 1024) + wchunk;
        auto issue_wo = [&](int i0) AINL {
#pragma unroll
            for (int i = i0; i < i0 + 4; ++i) glds16(wop + (size_t)i * 4096, ovo, lbase + hd * 16384 + i * 1024);
        };
        auto exps = [&](int t, f32x16& T, int a0, int a1, int a2, int a3) AINL {
            const int act[4] = {a0, a1, a2, a3};
#pragma unroll
            for (int c = 0; c < 4; ++c) {
                if (act[c] >= 0) pvh(act[c] >> 1, act[c] & 1);
#pragma unroll
                for (int i = 4 * c; i < 4 * c + 4; ++i) T[i] = __builtin_amdgcn_exp2f(T[i]);
                if (c == 1) P[t][0] = pack8(T, 0);
                if (c == 3) P[t][1] = pack8(T, 8);
                SB();
            }
            PIN(P[t][0]); PIN(P[t][1]);
        };
        {
            const bf16x8 k7 = kread(7);
            f32x16 S = MFMA32(k7, qf, zero);
            float m = fmaxf(S[0], S[1]);
#pragma unroll
            for (int i = 2; i < 16; i += 2) m = fmaxf(fmaxf(m, S[i]), S[i + 1]);
            const float nm = -(xhalf_max(m) + HEADROOM);
#pragma unroll
            for (int i = 0; i < 16; ++i) cn[i] = nm;
            T1 = MFMA32(k7, qf, cn);
        }
        T0 = MFMA32(kread(6), qf, cn);
        SB();
        exps(7, T1, -1, -1, -1, -1);
        T1 = MFMA32(kread(5), qf, cn);
        SB();
        exps(6, T0, -1, -1, -1, -1);
        T0 = MFMA32(kread(4), qf, cn);
        STAMP(13);
        if constexpr (L) {
            waitv<0>();
            __builtin_amdgcn_raw_buffer_store_b32(tagv, wsr, myflag, 0, 16);
            halo_check(1);
        }
        bar();
        STAMP(14);
        O = zero;
        vread(7); vread(6);
        if constexpr (L != (WO_G != 0)) issue_wo(0);
        SB();
        exps(5, T1, -1, 14, 15, 12);
        T1 = MFMA32(kread(3), qf, cn);
        vread(5); vread(4);
        if constexpr (L != (WO_G != 0)) issue_wo(4);
        SB();
        exps(4, T0, 13, -1, 10, 11);
        T0 = MFMA32(kread(2), qf, cn);
        vread(3);
        if constexpr (L != (WO_G != 0)) issue_wo(8);
        SB();
        exps(3, T1, 8, -1, 9, -1);
        T1 = MFMA32(kread(1), qf, cn);
        vread(2);
        if constexpr (L != (WO_G != 0)) issue_wo(12);
        SB();
        exps(2, T0, 6, -1, 7, -1);
        T0 = MFMA32(kread(0), qf, cn);
        Sb = MFMA32(kread(8), qf, cn);
        vread(1);
        ocf = flag_load(foff_o);
        SB();
        exps(1, T1, 4, -1, 5, -1);
        vread(0); vread(8);
        SB();
#pragma unroll
        for (int c = 0; c < 4; ++c) {
            if (c < 2) pvh(1, c);
#pragma unroll
            for (int i = 4 * c; i < 4 * c + 4; ++i) T0[i] = __builtin_amdgcn_exp2f(sel(Sb[i], T0[i], um[i]));
            if (c == 1) { const u32x4 p0 = pack8(T0, 0); P[0][0] = p0 & mk0; P[8][0] = p0 ^ P[0][0]; }
            if (c == 2) { pvh(0, 0); pvh(8, 0); }
            if (c == 3) { const u32x4 p1 = pack8(T0, 8); P[0][1] = p1 & mk1; P[8][1] = p1 ^ P[0][1]; }
            SB();
        }
        pvh(0, 1); pvh(8, 1);
        const float l = O[8];
        const float rl = __builtin_amdgcn_rcpf(l);
        float chk = 0.f;
#pragma unroll
        for (int i = 0; i < 8; ++i) { O[i] *= rl; chk += O[i]; }
        ov = pack8(O, 0);
        ok = __all(l < 0x1p126f && l > 0x1p-100f && __builtin_fabsf(chk) < __builtin_inff());
    };

    if (isL) {
        unsigned tag, kfl = 0;
        asm volatile("s_load_dword %0, %1, 0x0" : "=s"(tag) : "s"(ws) : "memory");
        STAMP(18);
        prepare();
#pragma unroll
        for (int n = 0; n < 16; ++n) {
            constexpr int FL = 12 - LOOK;
            const int last = n + LOOK - (n == 0 ? 0 : 1) < 15 ? n + LOOK - (n == 0 ? 0 : 1) : 15;
            const int young = 4 * (last - n) + ((n > FL && n <= 12) ? 1 : 0) + ((n > KH_AT && n <= (KH_AT + LOOK < 15 ? KH_AT + LOOK : 15)) ? 8 : 0);
            switch (young) {
#define WC(k) case k: waitv<k>(); break;
                WC(0) WC(4) WC(5) WC(8) WC(9) WC(12) WC(13) WC(16) WC(17) WC(20) WC(21)
#undef WC
                default: waitv<0>();
            }
            __builtin_amdgcn_s_barrier();
            if (n == 0) STAMP(20);
            if (n >= 1 && n + LOOK < 16) issue_step(n + LOOK);
            if (n == FL) kfl = flag_load(foff_h);
            if (n == KH_AT) {
                asm volatile("s_waitcnt lgkmcnt(0)" : "+s"(tag), "+v"(kfl) :: "memory");
                tagv = tag + 1u;
                flag_poll(kfl, foff_h);
                issue_halo(0);
            }
        }
        make_um();
        waitv<0>();
        issue_halo(1);
        __builtin_amdgcn_raw_buffer_store_b128(sentv, wsr, oslot, 0, 16);
        halo_check(0);
        STAMP(16);
        __builtin_amdgcn_s_barrier();
        STAMP(5);
        attention(IC<1>{});
    } else {
        const int ft = wave >> 1, tb = wave & 1;
        asm volatile("buffer_load_dword %0, off, %1, 0 sc1" : "=v"(tagv) : "s"(wsr) : "memory");
        const unsigned kb0 = (unsigned)((hg * 4 + 2 * ft) * 64 + ck * 2 + tb) * 1024;
        unsigned kdst = WS_K + kb0 + lane * 16;
        const int vsub = hh * 256 + (r32 & 15) * 16;
        unsigned vdst = WS_V + kb0 + (r32 >> 4) * 65536 + vsub;
        {
            const unsigned o1 = (lane & 15) + ((lane >> 4) & 1) >= nskip ? 0x3F803F80u : 0u;
            const u32x4 one = {o1, o1, o1, o1};
            *(LAS u32x4*)(lds + L_ONES + lane * 16) = one;
        }
        prepare();
        LAS char* xa[4];
        {
            const int u = hh ^ ((r32 >> 1) & 7);
#pragma unroll
            for (int ks = 0; ks < 4; ++ks) { xa[ks] = lds + L_RING + (tb * 32 + r32) * 128 + (((2 * ks) ^ u) << 4); PIN(xa[ks]); }
        }
        LAS char* wa0 = lds + (8 * hh + q4) * 128 + ((ft ^ (q4 >> 1)) << 6) + ((lane >> 4) & 1) * 32 + (lane & 3) * 8;
        LAS char* vl = lds + L_V + (2 * ft + (r32 >> 4)) * 10240 + (8 + tb) * 1024 + vsub;
        LAS char* kl = lds + L_K + (2 * ft) * 10240 + (8 + tb) * 1024 + lane * 16;
        LAS char* ql = lds + L_Q + (2 * ft) * 2048 + tb * 1024 + lane * 16;
        PIN(wa0); PIN(vdst); PIN(kdst); PIN(vl); PIN(kl); PIN(ql);
        auto wrd = [&](int off) AINL { return tr2(wa0 + off, wa0 + off + 512); };
        bf16x8 xf[32], wf[2][8];
        f32x16 acc = {};
#pragma unroll
        for (int n = 0; n <= 16; ++n) {
            const int slot = slot_of(n);
            const int p = n - 1;
            if (n < 16) {
                if (LOOK == 3 && n >= 1 && n + LOOK < 16) waitl0();
                __builtin_amdgcn_s_barrier();
                CFENCE();
                STAMP(30 + n);
            }
            if (p == 8 || p == 12) acc = f32x16{};
            SB();
#pragma unroll
            for (int j = 0; j < 8; ++j) {
                if (p >= 0 && p < 8 && j < 4) acc = MFMA32(wf[p & 1][j], xf[4 * p + j], acc);
                if (p >= 8 && p < 12) acc = MFMA32(xf[8 * (p - 8) + j], wf[p & 1][j], acc);
                if (p >= 12) acc = MFMA32(wf[p & 1][j], xf[8 * (p - 12) + j], acc);
                if (n < 8 && j < 4) {
                    xf[4 * n + j] = *(LAS bf16x8*)(xa[j] + slot);
                    wf[n & 1][j] = wrd(slot + 8192 + j * 2048);
                }
                if (n >= 8 && n < 16 && j < 4) {
                    wf[n & 1][2 * j] = wrd(slot + j * 4096);
                    wf[n & 1][2 * j + 1] = wrd(slot + j * 4096 + 2048);
                }
                SB();
            }
            if (n == 0) {
                __builtin_amdgcn_raw_buffer_store_b128(sentv, wsr, kdst, 0, 16);
                __builtin_amdgcn_raw_buffer_store_b128(sentv, wsr, kdst, 65536, 16);
                __builtin_amdgcn_raw_buffer_store_b128(sentv, wsr, vdst, 0, 16);
                __builtin_amdgcn_raw_buffer_store_b128(sentv, wsr, vdst + 512, 0, 16);
                __builtin_amdgcn_raw_buffer_store_b128(sentv, wsr, oslot, 0, 16);
            }
            if (p == 7) {
                const u32x4 k0 = sanitize(pack8(acc, 0)), k1 = sanitize(pack8(acc, 8));
                __builtin_amdgcn_raw_buffer_store_b128(k0, wsr, kdst, 0, 16);
                __builtin_amdgcn_raw_buffer_store_b128(k1, wsr, kdst, 65536, 16);
                *(LAS u32x4*)(kl) = k0;
                *(LAS u32x4*)(kl + 10240) = k1;
                STAMP(2);
            }
            if (p == 11) {
                const u32x4 v0 = sanitize(pack8(acc, 0)), v1 = sanitize(pack8(acc, 8));
                __builtin_amdgcn_raw_buffer_store_b128(v0, wsr, vdst, 0, 16);
                __builtin_amdgcn_raw_buffer_store_b128(v1, wsr, vdst + 512, 0, 16);
                *(LAS u32x4*)(vl) = v0;
                *(LAS u32x4*)(vl + 512) = v1;
                STAMP(3);
            }
            if (n == AFLAG_POS) {
                asm volatile("s_waitcnt vmcnt(0)" : "+v"(tagv) :: "memory");
                tagv += 1u;
                __builtin_amdgcn_raw_buffer_store_b32(tagv, wsr, myflag, 0, 16);
                STAMP(26);
            }
            if (n == 14) make_um();
        }
#pragma unroll
        for (int i = 0; i < 16; ++i) acc[i] *= CEXP;
        *(LAS u32x4*)(ql) = pack8(acc, 0);
        *(LAS u32x4*)(ql + 2048) = pack8(acc, 8);
        STAMP(4);
        bar();
        STAMP(5);
        attention(IC<0>{});
    }
    STAMP(25);
    if (UNLIKELY(!ok)) { STAMP(15); ov = unit_exact(lds, hd, qb, nskip, lane); }
    __builtin_amdgcn_raw_buffer_store_b128(sanitize(ov), wsr, oslot, 0, 16);
    STAMP(6);

    {
        asm volatile("s_waitcnt vmcnt(1)" : "+v"(ocf) :: "memory");
        bar();
        flag_poll(ocf, foff_o);
        bf16x8 wa[8][2];
        {
            LAS char* wb2 = lds + (int)((unsigned)(wb - lds) ^ 64u);
#pragma unroll
            for (int j = 0; j < 8; ++j) {
                wa[j][0] = tr2(wb + j * 2048, wb + j * 2048 + 1024);
                wa[j][1] = tr2(wb2 + j * 2048, wb2 + j * 2048 + 1024);
            }
        }
        u32x4 d[8];
        {
            const unsigned pofs = osrc - lane * 16 + (lane & 7) * 1024 + 1020;
            unsigned got = 0;
#pragma unroll 1
            for (int spin = 0; spin < SPIN_CAP; ++spin) {
                CFENCE();
                const unsigned pr = __builtin_amdgcn_raw_buffer_load_b32(wsr, pofs, 0, 16);
                const unsigned rdy = (unsigned)__builtin_amdgcn_ballot_w64(pr != SENT) & 0xFFu;
                const unsigned nw = rdy & ~got;
#pragma unroll
                for (int j = 0; j < 8; ++j)
                    if (nw >> j & 1) d[j] = __builtin_amdgcn_raw_buffer_load_b128(wsr, osrc + j * 1024, 0, 16);
                got |= rdy;
                if (got == 0xFFu) break;
                if (spin == 0) STAMP(7);
                if (__builtin_popcount(got) >= 8 - DIRECT_TAIL) {
#pragma unroll 1
                    for (; spin < SPIN_CAP; ++spin) {
                        CFENCE();
#pragma unroll
                        for (int j = 0; j < 8; ++j)
                            if (!(got >> j & 1)) d[j] = __builtin_amdgcn_raw_buffer_load_b128(wsr, osrc + j * 1024, 0, 16);
#pragma unroll
                        for (int j = 0; j < 8; ++j)
                            if (!(got >> j & 1)) got |= __all(d[j].w != SENT) ? 1u << j : 0u;
                        if (got == 0xFFu) break;
                    }
                    break;
                }
                __builtin_amdgcn_s_sleep(PROBE_SLEEP);
            }
        }
        f32x16 c0 = {}, c1 = {};
        unsigned mx = 0;
#pragma unroll
        for (int j = 0; j < 8; ++j) {
            c0 = MFMA32(wa[j][0], as_bf(d[j]), c0);
            c1 = MFMA32(wa[j][1], as_bf(d[j]), c1);
            mx = max(mx, d[j].w);
        }
        if (UNLIKELY(!__all(mx != SENT))) {
#pragma unroll 1
            for (int spin = 0; spin < SPIN_CAP; ++spin) {
                bool pend = false;
                CFENCE();
#pragma unroll
                for (int j = 0; j < 8; ++j)
                    if (!__all(d[j].w != SENT)) { d[j] = __builtin_amdgcn_raw_buffer_load_b128(wsr, osrc + j * 1024, 0, 16); pend = true; }
                if (!pend) break;
            }
            c0 = f32x16{}; c1 = f32x16{};
#pragma unroll
            for (int j = 0; j < 8; ++j) {
                c0 = MFMA32(wa[j][0], as_bf(d[j]), c0);
                c1 = MFMA32(wa[j][1], as_bf(d[j]), c1);
            }
        }
        STAMP(8);
        if (UNLIKELY(bid == 0 && wave == 7)) {
#pragma unroll 1
            for (int spin = 0; spin < SPIN_CAP; ++spin) {
                CFENCE();
                bool all = true;
#pragma unroll
                for (int i = 0; i < 8; ++i) {
                    const u32x4 f = __builtin_amdgcn_raw_buffer_load_b128(wsr, WS_FLAG + ((i >> 1) * 64 + lane) * 128 + (i & 1) * 16, 0, 16);
                    all &= f.x == tagv && f.y == tagv && f.z == tagv && f.w == tagv;
                }
                if (__all(all)) break;
            }
            if (lane == 0) __builtin_amdgcn_raw_buffer_store_b32(tagv, wsr, WS_EPOCH, 0, 16);
        }
#pragma unroll
        for (int gq = 0; gq < 4; ++gq) {
            f32x4 a = {c0[4 * gq], c0[4 * gq + 1], c0[4 * gq + 2], c0[4 * gq + 3]};
            f32x4 b = {c1[4 * gq], c1[4 * gq + 1], c1[4 * gq + 2], c1[4 * gq + 3]};
            *(LAS f32x4*)(rpw + gq * 32) = a;
            *(LAS f32x4*)(rpw + 4608 + gq * 32) = b;
        }
        bar();
        STAMP(9);
        {
            f32x4 a = *(LAS f32x4*)(rpr), b = *(LAS f32x4*)(rpr + 16);
#pragma unroll
            for (int s = 1; s < 4; ++s) { a += *(LAS f32x4*)(rpr + s * 9216); b += *(LAS f32x4*)(rpr + s * 9216 + 16); }
            const u32x4 r = {pack2(a[0], a[1]), pack2(a[2], a[3]), pack2(b[0], b[1]), pack2(b[2], b[3])};
            asm volatile("global_store_dwordx4 %0, %1, off sc1\n\ts_nop 1" :: "v"(dst), "v"(r) : "memory");
        }
        STAMP(10); CSTAMP(63); STAMP_FLUSH();
    }
}

extern "C" void kernel_launch(void* const* d_in, const int* in_sizes, int n_in, void* d_out, int out_size, void* d_ws,
                              size_t ws_size, hipStream_t stream) {
    (void)in_sizes; (void)n_in; (void)out_size; (void)ws_size;
    (void)hipFuncSetAttribute((const void*)swa_block, hipFuncAttributeMaxDynamicSharedMemorySize, LDS_BYTES);
    swa_block<<<256, 512, LDS_BYTES, stream>>>((const u16*)d_in[0], (const u16*)d_in[1], (const u16*)d_in[2], (u16*)d_out, (char*)d_ws);
}
```

```cpp
#include <hip/hip_runtime.h>
#include <stdint.h>
#include <type_traits>

typedef short bf16x8 __attribute__((ext_vector_type(8)));
typedef short s16x4 __attribute__((ext_vector_type(4)));
typedef float f32x16 __attribute__((ext_vector_type(16)));
typedef float f32x4 __attribute__((ext_vector_type(4)));
typedef float f32x2 __attribute__((ext_vector_type(2)));
typedef unsigned u32x4 __attribute__((ext_vector_type(4)));
typedef unsigned u32x2 __attribute__((ext_vector_type(2)));
typedef __bf16 bf16x2 __attribute__((ext_vector_type(2)));
typedef unsigned short u16;
typedef unsigned long long u64;
#define LAS __attribute__((address_space(3)))
#define DI __device__ __forceinline__

#define AFLAG_POS 6
#define PROBE_SLEEP 2
#define DIRECT_TAIL 4
#define LOOK 3
#define WO_G 1
#define KH_AT 13

constexpr unsigned WS_EPOCH = 0;
constexpr unsigned WS_FLAG = 1024;
constexpr unsigned WS_K = 65536;
constexpr unsigned WS_V = WS_K + 32 * 64 * 1024;
constexpr unsigned WS_O = WS_V + 32 * 64 * 1024;
constexpr unsigned WS_END = WS_O + 64 * 32 * 1024;
constexpr int L_RING = 0;
constexpr int L_K = 65536;
constexpr int L_V = L_K + 40960;
constexpr int L_Q = L_V + 40960;
constexpr int L_ONES = L_Q + 8192;
constexpr int L_RED = L_K;
constexpr int LDS_BYTES = L_ONES + 1024;
constexpr int slot_of(int n) { return (n & 3) * 16384; }

constexpr float CEXP = 0.36067376022224085f;
constexpr float HEADROOM = 32.f;
constexpr unsigned SENT = 0xFFFFFFFFu;
constexpr unsigned OOB = 0x40000000u;
constexpr int SPIN_CAP = 1 << 18;

DI unsigned pack2(float a, float b) {
    f32x2 v = {a, b};
    return __builtin_bit_cast(unsigned, __builtin_convertvector(v, bf16x2));
}
DI u32x4 pack8(const f32x16& a, int o) {
    u32x4 r = {pack2(a[o], a[o + 1]), pack2(a[o + 2], a[o + 3]), pack2(a[o + 4], a[o + 5]), pack2(a[o + 6], a[o + 7])};
    return r;
}
DI u32x4 sanitize(u32x4 v) {
    v.w = min(v.w, SENT - 1);
    return v;
}
DI bf16x8 as_bf(u32x4 v) { return __builtin_bit_cast(bf16x8, v); }
DI u32x4 as_u(bf16x8 v) { return __builtin_bit_cast(u32x4, v); }
DI void waitl0() { asm volatile("" ::: "memory"); __builtin_amdgcn_s_waitcnt(0xC07F); asm volatile("" ::: "memory"); }
DI void bar() { waitl0(); __builtin_amdgcn_s_barrier(); asm volatile("" ::: "memory"); }
template <int N> DI void waitv() { asm volatile("s_waitcnt vmcnt(%0)" :: "i"(N) : "memory"); }
#define CFENCE() asm volatile("" ::: "memory")
#define SB() __builtin_amdgcn_sched_barrier(0)
#define PIN(x) asm volatile("" : "+v"(x))
#define MFMA32(a, b, c) __builtin_amdgcn_mfma_f32_32x32x16_bf16((a), (b), (c), 0, 0, 0)
DI s16x4 trread(LAS char* p) { return __builtin_amdgcn_ds_read_tr16_b64_v4i16((LAS s16x4*)p); }
DI bf16x8 tr2(LAS char* p0, LAS char* p1) {
    s16x4 a = trread(p0), b = trread(p1);
    return __builtin_shufflevector(a, b, 0, 1, 2, 3, 4, 5, 6, 7);
}
DI void glds16(const void* sbase, unsigned voff, unsigned ldsaddr) {
    asm volatile("s_mov_b32 m0, %0\n\ts_nop 0\n\tglobal_load_lds_dwordx4 %1, %2" :: "s"(ldsaddr), "v"(voff), "s"(sbase) : "memory");
}
DI void blds16_sc1(__amdgpu_buffer_rsrc_t rs, unsigned voff, unsigned soff, unsigned ldsaddr) {
    asm volatile("s_mov_b32 m0, %0\n\ts_nop 0\n\tbuffer_load_dwordx4 %1, %2, %3 offen sc1 lds" :: "s"(ldsaddr), "v"(voff), "s"(rs), "s"(soff) : "memory");
}
DI float xhalf_max(float m) {
    auto r = __builtin_amdgcn_permlane32_swap(__float_as_uint(m), __float_as_uint(m), false, false);
    return fmaxf(__uint_as_float(r[0]), __uint_as_float(r[1]));
}
DI unsigned min4(u32x4 f) { return __builtin_amdgcn_readfirstlane(min(min(f.x, f.y), min(f.z, f.w))); }
DI u32x4 ldflag4(LAS char* p) { CFENCE(); u32x4 f = *(volatile LAS u32x4*)p; CFENCE(); return f; }
DI unsigned ldflag1(LAS char* p) { CFENCE(); unsigned f = *(volatile LAS unsigned*)p; CFENCE(); return __builtin_amdgcn_readfirstlane(f); }
#define UNLIKELY(c) __builtin_expect(!!(c), 0)
#define AINL __attribute__((always_inline))
template <int V> using IC = std::integral_constant<int, V>;
DI void wait4_ge(LAS char* p, unsigned n) {
    if (UNLIKELY(min4(ldflag4(p)) < n)) {
#pragma unroll 1
        for (int spin = 0; spin < SPIN_CAP; ++spin) if (min4(ldflag4(p)) >= n) break;
    }
}
DI void wait1_ge(LAS char* p, unsigned n) {
    if (UNLIKELY(ldflag1(p) < n)) {
#pragma unroll 1
        for (int spin = 0; spin < SPIN_CAP; ++spin) if (ldflag1(p) >= n) break;
    }
}
DI void blds4k_sc1(__amdgpu_buffer_rsrc_t rs, unsigned voff, unsigned soff, unsigned ldsaddr) {
    asm volatile("s_mov_b32 m0, %0\n\ts_nop 0\n\tbuffer_load_dwordx4 %1, %2, %3 offen sc1 lds\n\t"
                 "buffer_load_dwordx4 %1, %2, %3 offen offset:1024 sc1 lds\n\t"
                 "buffer_load_dwordx4 %1, %2, %3 offen offset:2048 sc1 lds\n\t"
                 "buffer_load_dwordx4 %1, %2, %3 offen offset:3072 sc1 lds" :: "s"(ldsaddr), "v"(voff), "s"(rs), "s"(soff) : "memory");
}
DI void stflag(LAS char* p, unsigned v) { waitl0(); *(volatile LAS unsigned*)p = v; CFENCE(); }
constexpr u64 upper_mask(int i) {
    u64 m = 0;
    for (int l = 0; l < 64; ++l) if ((i & 3) + 8 * (i >> 2) + 4 * (l >> 5) > (l & 31)) m |= 1ull << l;
    return m;
}
DI float sel(float lo, float hi, u64 m) { return __builtin_amdgcn_inverse_ballot_w64(m) ? hi : lo; }

#define STAMP(i) do {} while (0)
#define CSTAMP(i) do {} while (0)
#define STAMP_FLUSH() do {} while (0)

DI u32x4 unit_exact(LAS char* lds, int hd, int qb, int nskip, int lane) {
    const int r32 = lane & 31, hh = lane >> 5;
    const bf16x8 qf = *(LAS bf16x8*)(lds + L_Q + hd * 2048 + qb * 1024 + lane * 16);
    LAS char* kp = lds + L_K + hd * 10240 + qb * 1024 + lane * 16;
    const int va0 = (r32 < 16) ? (L_V + hd * 10240 + qb * 1024 + hh * 256 + r32 * 16) : (L_ONES + qb * 256);
    const int vstr = (r32 < 16) ? 1024 : 16;
    const float NINF = -__builtin_inff();
    const int t0 = nskip > qb ? nskip - qb : 0;
    const f32x16 zero = {};
    float m = NINF;
#pragma unroll 1
    for (int t = t0; t < 9; ++t) {
        f32x16 S = MFMA32(*(LAS bf16x8*)(kp + t * 1024), qf, zero);
#pragma unroll
        for (int i = 0; i < 16; ++i) {
            const int key = (i & 3) + 8 * (i >> 2) + 4 * hh;
            const bool ok = t == 0 ? key > r32 : t == 8 ? key <= r32 : true;
            m = fmaxf(m, ok ? S[i] : NINF);
        }
    }
    m = xhalf_max(m);
    f32x16 O = {};
#pragma unroll 1
    for (int t = t0; t < 9; ++t) {
        f32x16 S = MFMA32(*(LAS bf16x8*)(kp + t * 1024), qf, zero);
#pragma unroll
        for (int i = 0; i < 16; ++i) {
            const int key = (i & 3) + 8 * (i >> 2) + 4 * hh;
            const bool ok = t == 0 ? key > r32 : t == 8 ? key <= r32 : true;
            S[i] = ok ? __builtin_amdgcn_exp2f(S[i] - m) : 0.f;
        }
        LAS char* vp = lds + va0 + t * vstr;
        O = MFMA32(*(LAS bf16x8*)(vp), as_bf(pack8(S, 0)), O);
        O = MFMA32(*(LAS bf16x8*)(vp + 512), as_bf(pack8(S, 8)), O);
    }
    const float rl = __builtin_amdgcn_rcpf(O[8]);
#pragma unroll
    for (int i = 0; i < 8; ++i) O[i] *= rl;
    return pack8(O, 0);
}

__global__ __launch_bounds__(512, 2) void swa_block(const u16* __restrict__ x, const u16* __restrict__ wqkv,
                                                    const u16* __restrict__ wo, u16* __restrict__ out, char* ws) {
    extern __shared__ __attribute__((aligned(16))) char smem_[];
    LAS char* lds = (LAS char*)smem_;
    asm volatile("" :: "s"(x), "s"(wqkv), "s"(wo), "s"(out), "s"(ws));
    const int tid = threadIdx.x;
    const int wave = __builtin_amdgcn_readfirstlane(tid >> 6);
    const int lane = tid & 63, r32 = lane & 31, hh = lane >> 5;
    const int bid = blockIdx.x, g = bid & 7, ii = bid >> 3;
    const int hg = (g & 1) * 4 + (ii & 3);
    const int ck = (g >> 1) * 8 + (ii >> 2);
    const int rot = ((ii >> 2) + 2 * (ii & 3)) & 7;
    const unsigned lbase = (unsigned)(uintptr_t)lds;
    const int hd = wave & 3;
    const bool isL = wave >= 4;
    unsigned stampv = 0; (void)stampv;
    STAMP(0); CSTAMP(62);
    const unsigned wchunk = (unsigned)(((((lane >> 2) & 1) ^ ((lane >> 4) & 1)) * 4 + (lane & 3)) * 16);
    const unsigned xvo = (unsigned)((lane >> 3) * 512 + (((lane & 7) ^ (lane >> 4) ^ ((hd & 1) << 2)) * 8)) * 2u;
    const unsigned wvo = (unsigned)((lane >> 3) * 3072) + wchunk;
    const u16* xsrc = x + (size_t)(ck * 64 + hd * 8) * 512;
    const u16* wsrc = wqkv + (size_t)(hd * 8) * 1536 + hg * 64;
    auto issue_w = [&](int s, int col, unsigned d) AINL {
        const u16* wp = wsrc + (size_t)((s + rot) & 7) * 64 * 1536 + col;
        glds16(wp, wvo, d);
        glds16(wp + 32 * 1536, wvo, d + 4096);
    };
    auto issue_step = [&](int n) AINL {
        const unsigned d = lbase + L_RING + slot_of(n) + hd * 1024;
        if (n < 8) {
            const u16* xp = xsrc + ((n + rot) & 7) * 64;
            glds16(xp, xvo, d);
            glds16(xp + 32 * 512, xvo, d + 4096);
            issue_w(n, 512, d + 8192);
        } else {
            issue_w(2 * (n & 3), n < 12 ? 1024 : 0, d);
            issue_w(2 * (n & 3) + 1, n < 12 ? 1024 : 0, d + 8192);
        }
    };
    if (__builtin_expect(isL, 1)) { issue_step(0); issue_step(1); issue_step(2); if (LOOK == 3) issue_step(3); }
    const int orow = g * 256 + (ii >> 3) * 64, ocol = (ii & 7) * 64;
    const __amdgpu_buffer_rsrc_t wsr = __builtin_amdgcn_make_buffer_rsrc(ws, 0, WS_END, 0x00020000);
    const int hbase = 2 * ck - 8;
    const int nskip = hbase < 0 ? -hbase : 0;
    const int q4 = (lane & 15) >> 2;
    const int qb = isL ? 1 : 0;
    const unsigned myflag = WS_FLAG + (unsigned)(hg * 32 + ck) * 128 + wave * 4;
    const u32x4 sentv = {SENT, SENT, SENT, SENT};
    unsigned tagv;

    u32x4 mk0, mk1;
    u64 um[16];
    unsigned foff_h, foff_o;
    unsigned hvo, oslot, osrc;
    LAS char *kp, *vp0, *qp, *hchk, *wb, *rpw, *rpr;
    int vstr;
    u16* dst;
    auto prepare = [&]() AINL {
#pragma unroll
        for (int j = 0; j < 4; ++j) {
            const int ka = ((2 * j) & 3) + 8 * ((2 * j) >> 2) + 4 * hh;
            mk0[j] = ((ka > r32) ? 0xFFFFu : 0u) | ((ka + 1 > r32) ? 0xFFFF0000u : 0u);
            mk1[j] = ((ka + 16 > r32) ? 0xFFFFu : 0u) | ((ka + 17 > r32) ? 0xFFFF0000u : 0u);
        }
        PIN(mk0); PIN(mk1);
        const int l8 = lane & 7;
        const int pc = ck - 1 - (l8 >> 1);
        foff_h = WS_FLAG + (unsigned)(hg * 32 + (pc < 0 ? ck : pc)) * 128 + ((hd >> 1) * 2 + (l8 & 1)) * 4;
        foff_o = WS_FLAG + (unsigned)((2 * hd + (l8 >> 2)) * 32 + (orow >> 6)) * 128 + (qb * 4 + (l8 & 3)) * 4;
        hvo = (unsigned)((int)WS_K + ((hg * 4 + hd) * 64 + hbase) * 1024 + lane * 16);
        hchk = lds + L_K + hd * 10240 + lane * 16 + 12;
        osrc = WS_O + (unsigned)(((orow >> 5) + qb) * 32 + hd * 8) * 1024 + lane * 16;
        kp = lds + L_K + hd * 10240 + qb * 1024 + lane * 16;
        qp = lds + L_Q + hd * 2048 + qb * 1024 + lane * 16;
        vp0 = lds + ((r32 < 16) ? (L_V + hd * 10240 + qb * 1024 + hh * 256 + r32 * 16) : (L_ONES + qb * 256));
        vstr = (r32 < 16) ? 1024 : 16;
        wb = lds + hd * 16384 + (4 * hh + q4) * 128 + ((lane >> 4) & 1) * 32 + (lane & 3) * 8 + ((q4 >> 1) << 6);
        rpw = lds + L_RED + wave * 9216 + r32 * 144 + hh * 16;
        const int qr = hd * 8 + (lane >> 3), co = lane & 7;
        rpr = lds + L_RED + (wave & 4) * 9216 + (co >> 2) * 4608 + qr * 144 + (co & 3) * 32;
        dst = out + (size_t)(orow + qb * 32 + qr) * 512 + ocol + co * 8;
        PIN(foff_h); PIN(foff_o); PIN(hvo); PIN(hchk); PIN(osrc); PIN(kp); PIN(qp); PIN(vp0); PIN(vstr); PIN(wb); PIN(rpw); PIN(rpr); PIN(dst);
    };
    oslot = WS_O + (unsigned)((ck * 2 + qb) * 32 + hg * 4 + hd) * 1024 + lane * 16;
    auto make_um = [&]() AINL {
#pragma unroll
        for (int i = 0; i < 16; ++i) { um[i] = upper_mask(i); asm volatile("" : "+s"(um[i])); }
    };
    const unsigned hdst = lbase + L_K + hd * 10240;
    auto issue_halo = [&](int v) AINL {
#pragma unroll
        for (int i = 0; i < 8; ++i) blds16_sc1(wsr, hvo, i >= nskip ? i * 1024 + v * (WS_V - WS_K) : OOB, hdst + v * 40960 + i * 1024);
    };
    auto halo_probe = [&](int v) AINL -> bool {
        unsigned mx = 0;
        CFENCE();
#pragma unroll
        for (int t = 0; t < 8; ++t) mx = max(mx, *(LAS unsigned*)(hchk + v * 40960 + t * 1024));
        return __all(mx != SENT);
    };
    auto halo_check = [&](int v) AINL {
        if (UNLIKELY(!halo_probe(v))) {
            STAMP(28);
#pragma unroll 1
            for (int spin = 0; spin < SPIN_CAP; ++spin) {
#pragma unroll 1
                for (int i = 0; i < 8; ++i)
                    blds16_sc1(wsr, hvo, __builtin_amdgcn_readfirstlane(i >= nskip ? i * 1024 + v * (WS_V - WS_K) : OOB), __builtin_amdgcn_readfirstlane(hdst + v * 40960 + i * 1024));
                waitv<0>();
                if (halo_probe(v)) break;
            }
        }
    };
    auto flag_load = [&](unsigned off) AINL -> unsigned {
        unsigned f;
        asm volatile("buffer_load_dword %0, %1, %2, 0 offen sc1" : "=v"(f) : "v"(off), "s"(wsr) : "memory");
        return f;
    };
    auto flag_poll = [&](unsigned f, unsigned off) AINL {
        if (UNLIKELY(!__all(f == tagv))) {
#pragma unroll 1
            for (int spin = 0; spin < SPIN_CAP; ++spin) {
                __builtin_amdgcn_s_sleep(1);
                asm volatile("buffer_load_dword %0, %1, %2, 0 offen sc1\n\ts_waitcnt vmcnt(0)" : "=v"(f) : "v"(off), "s"(wsr) : "memory");
                if (__all(f == tagv)) break;
            }
        }
    };
    unsigned ocf = 0;
    u32x4 ov;
    bool ok;

    auto attention = [&]() AINL {
        const bool L = isL;
        u32x4 P[9][2];
        f32x16 cn, T0, T1, Sb, O;
        const f32x16 zero = {};
        const bf16x8 qf = *(LAS bf16x8*)qp;
        auto kread = [&](int t) AINL { return *(LAS bf16x8*)(kp + t * 1024); };
        bf16x8 vf[9][2];
        auto vread = [&](int t) AINL { LAS char* vp = vp0 + t * vstr; vf[t][0] = *(LAS bf16x8*)(vp); vf[t][1] = *(LAS bf16x8*)(vp + 512); };
        auto pvh = [&](int t, int h) AINL { O = MFMA32(vf[t][h], as_bf(P[t][h]), O); };
        const u16* wop = wo + (size_t)(hd * 128) * 512 + ocol;
        const unsigned ovo = (unsigned)((lane >> 3) * 1024) + wchunk;
        const int wo0 = qb * 8;
        auto issue_wo = [&](int i0) AINL {
#pragma unroll
            for (int i = i0; i < i0 + 2; ++i) glds16(wop + (size_t)(wo0 + i) * 4096, ovo, lbase + hd * 16384 + (wo0 + i) * 1024);
        };
        auto exps = [&](int t, f32x16& T, int a0, int a1, int a2, int a3) AINL {
            const int act[4] = {a0, a1, a2, a3};
#pragma unroll
            for (int c = 0; c < 4; ++c) {
                if (act[c] >= 0) pvh(act[c] >> 1, act[c] & 1);
#pragma unroll
                for (int i = 4 * c; i < 4 * c + 4; ++i) T[i] = __builtin_amdgcn_exp2f(T[i]);
                if (c == 1) P[t][0] = pack8(T, 0);
                if (c == 3) P[t][1] = pack8(T, 8);
                SB();
            }
            PIN(P[t][0]); PIN(P[t][1]);
        };
        {
            const bf16x8 k7 = kread(7);
            f32x16 S = MFMA32(k7, qf, zero);
            float m = fmaxf(S[0], S[1]);
#pragma unroll
            for (int i = 2; i < 16; i += 2) m = fmaxf(fmaxf(m, S[i]), S[i + 1]);
            const float nm = -(xhalf_max(m) + HEADROOM);
#pragma unroll
            for (int i = 0; i < 16; ++i) cn[i] = nm;
            T1 = MFMA32(k7, qf, cn);
        }
        T0 = MFMA32(kread(6), qf, cn);
        SB();
        exps(7, T1, -1, -1, -1, -1);
        T1 = MFMA32(kread(5), qf, cn);
        SB();
        exps(6, T0, -1, -1, -1, -1);
        T0 = MFMA32(kread(4), qf, cn);
        STAMP(13);
        if (L) {
            waitv<0>();
            __builtin_amdgcn_raw_buffer_store_b32(tagv, wsr, myflag, 0, 16);
            halo_check(1);
        }
        bar();
        STAMP(14);
        O = zero;
        vread(7); vread(6);
        issue_wo(0);
        SB();
        exps(5, T1, -1, 14, 15, 12);
        T1 = MFMA32(kread(3), qf, cn);
        vread(5); vread(4);
        issue_wo(2);
        SB();
        exps(4, T0, 13, -1, 10, 11);
        T0 = MFMA32(kread(2), qf, cn);
        vread(3);
        issue_wo(4);
        SB();
        exps(3, T1, 8, -1, 9, -1);
        T1 = MFMA32(kread(1), qf, cn);
        vread(2);
        issue_wo(6);
        SB();
        exps(2, T0, 6, -1, 7, -1);
        T0 = MFMA32(kread(0), qf, cn);
        Sb = MFMA32(kread(8), qf, cn);
        vread(1);
        ocf = flag_load(foff_o);
        SB();
        exps(1, T1, 4, -1, 5, -1);
        vread(0); vread(8);
        SB();
#pragma unroll
        for (int c = 0; c < 4; ++c) {
            if (c < 2) pvh(1, c);
#pragma unroll
            for (int i = 4 * c; i < 4 * c + 4; ++i) T0[i] = __builtin_amdgcn_exp2f(sel(Sb[i], T0[i], um[i]));
            if (c == 1) { const u32x4 p0 = pack8(T0, 0); P[0][0] = p0 & mk0; P[8][0] = p0 ^ P[0][0]; }
            if (c == 2) { pvh(0, 0); pvh(8, 0); }
            if (c == 3) { const u32x4 p1 = pack8(T0, 8); P[0][1] = p1 & mk1; P[8][1] = p1 ^ P[0][1]; }
            SB();
        }
        pvh(0, 1); pvh(8, 1);
        const float l = O[8];
        const float rl = __builtin_amdgcn_rcpf(l);
        float chk = 0.f;
#pragma unroll
        for (int i = 0; i < 8; ++i) { O[i] *= rl; chk += O[i]; }
        ov = pack8(O, 0);
        ok = __all(l < 0x1p126f && l > 0x1p-100f && __builtin_fabsf(chk) < __builtin_inff());
    };

    prepare();
    if (isL) {
        unsigned tag, kfl = 0;
        asm volatile("s_load_dword %0, %1, 0x0" : "=s"(tag) : "s"(ws) : "memory");
        STAMP(18);
#pragma unroll
        for (int n = 0; n < 16; ++n) {
            constexpr int FL = 12 - LOOK;
            const int last = n + LOOK - (n == 0 ? 0 : 1) < 15 ? n + LOOK - (n == 0 ? 0 : 1) : 15;
            const int young = 4 * (last - n) + ((n > FL && n <= 12) ? 1 : 0) + ((n > KH_AT && n <= (KH_AT + LOOK < 15 ? KH_AT + LOOK : 15)) ? 8 : 0);
            switch (young) {
#define WC(k) case k: waitv<k>(); break;
                WC(0) WC(4) WC(5) WC(8) WC(9) WC(12) WC(13) WC(16) WC(17) WC(20) WC(21)
#undef WC
                default: waitv<0>();
            }
            __builtin_amdgcn_s_barrier();
            if (n == 0) STAMP(20);
            if (n >= 1 && n + LOOK < 16) issue_step(n + LOOK);
            if (n == FL) kfl = flag_load(foff_h);
            if (n == KH_AT) {
                asm volatile("s_waitcnt lgkmcnt(0)" : "+s"(tag), "+v"(kfl) :: "memory");
                tagv = tag + 1u;
                flag_poll(kfl, foff_h);
                issue_halo(0);
            }
        }
        waitv<0>();
        issue_halo(1);
        __builtin_amdgcn_raw_buffer_store_b128(sentv, wsr, oslot, 0, 16);
        halo_check(0);
        STAMP(16);
    } else {
        const int ft = wave >> 1, tb = wave & 1;
        asm volatile("buffer_load_dword %0, off, %1, 0 sc1" : "=v"(tagv) : "s"(wsr) : "memory");
        const unsigned kb0 = (unsigned)((hg * 4 + 2 * ft) * 64 + ck * 2 + tb) * 1024;
        unsigned kdst = WS_K + kb0 + lane * 16;
        const int vsub = hh * 256 + (r32 & 15) * 16;
        unsigned vdst = WS_V + kb0 + (r32 >> 4) * 65536 + vsub;
        {
            const unsigned o1 = (lane & 15) + ((lane >> 4) & 1) >= nskip ? 0x3F803F80u : 0u;
            const u32x4 one = {o1, o1, o1, o1};
            *(LAS u32x4*)(lds + L_ONES + lane * 16) = one;
        }
        LAS char* xa[4];
        {
            const int u = hh ^ ((r32 >> 1) & 7);
#pragma unroll
            for (int ks = 0; ks < 4; ++ks) { xa[ks] = lds + L_RING + (tb * 32 + r32) * 128 + (((2 * ks) ^ u) << 4); PIN(xa[ks]); }
        }
        LAS char* wa0 = lds + (8 * hh + q4) * 128 + ((ft ^ (q4 >> 1)) << 6) + ((lane >> 4) & 1) * 32 + (lane & 3) * 8;
        LAS char* vl = lds + L_V + (2 * ft + (r32 >> 4)) * 10240 + (8 + tb) * 1024 + vsub;
        LAS char* kl = lds + L_K + (2 * ft) * 10240 + (8 + tb) * 1024 + lane * 16;
        LAS char* ql = lds + L_Q + (2 * ft) * 2048 + tb * 1024 + lane * 16;
        PIN(wa0); PIN(vdst); PIN(kdst); PIN(vl); PIN(kl); PIN(ql);
        auto wrd = [&](int off) AINL { return tr2(wa0 + off, wa0 + off + 512); };
        bf16x8 xf[32], wf[2][8];
        f32x16 acc = {};
#pragma unroll
        for (int n = 0; n <= 16; ++n) {
            const int slot = slot_of(n);
            const int p = n - 1;
            if (n < 16) {
                if (LOOK == 3 && n >= 1 && n + LOOK < 16) waitl0();
                __builtin_amdgcn_s_barrier();
                CFENCE();
                STAMP(30 + n);
            }
            if (p == 8 || p == 12) acc = f32x16{};
            SB();
#pragma unroll
            for (int j = 0; j < 8; ++j) {
                if (p >= 0 && p < 8 && j < 4) acc = MFMA32(wf[p & 1][j], xf[4 * p + j], acc);
                if (p >= 8 && p < 12) acc = MFMA32(xf[8 * (p - 8) + j], wf[p & 1][j], acc);
                if (p >= 12) acc = MFMA32(wf[p & 1][j], xf[8 * (p - 12) + j], acc);
                if (n < 8 && j < 4) {
                    xf[4 * n + j] = *(LAS bf16x8*)(xa[j] + slot);
                    wf[n & 1][j] = wrd(slot + 8192 + j * 2048);
                }
                if (n >= 8 && n < 16 && j < 4) {
                    wf[n & 1][2 * j] = wrd(slot + j * 4096);
                    wf[n & 1][2 * j + 1] = wrd(slot + j * 4096 + 2048);
                }
                SB();
            }
            if (n == 0) {
                __builtin_amdgcn_raw_buffer_store_b128(sentv, wsr, kdst, 0, 16);
                __builtin_amdgcn_raw_buffer_store_b128(sentv, wsr, kdst, 65536, 16);
                __builtin_amdgcn_raw_buffer_store_b128(sentv, wsr, vdst, 0, 16);
                __builtin_amdgcn_raw_buffer_store_b128(sentv, wsr, vdst + 512, 0, 16);
                __builtin_amdgcn_raw_buffer_store_b128(sentv, wsr, oslot, 0, 16);
            }
            if (p == 7) {
                const u32x4 k0 = sanitize(pack8(acc, 0)), k1 = sanitize(pack8(acc, 8));
                __builtin_amdgcn_raw_buffer_store_b128(k0, wsr, kdst, 0, 16);
                __builtin_amdgcn_raw_buffer_store_b128(k1, wsr, kdst, 65536, 16);
                *(LAS u32x4*)(kl) = k0;
                *(LAS u32x4*)(kl + 10240) = k1;
                STAMP(2);
            }
            if (p == 11) {
                const u32x4 v0 = sanitize(pack8(acc, 0)), v1 = sanitize(pack8(acc, 8));
                __builtin_amdgcn_raw_buffer_store_b128(v0, wsr, vdst, 0, 16);
                __builtin_amdgcn_raw_buffer_store_b128(v1, wsr, vdst + 512, 0, 16);
                *(LAS u32x4*)(vl) = v0;
                *(LAS u32x4*)(vl + 512) = v1;
                STAMP(3);
            }
            if (n == AFLAG_POS) {
                asm volatile("s_waitcnt vmcnt(0)" : "+v"(tagv) :: "memory");
                tagv += 1u;
                __builtin_amdgcn_raw_buffer_store_b32(tagv, wsr, myflag, 0, 16);
                STAMP(26);
            }
        }
#pragma unroll
        for (int i = 0; i < 16; ++i) acc[i] *= CEXP;
        *(LAS u32x4*)(ql) = pack8(acc, 0);
        *(LAS u32x4*)(ql + 2048) = pack8(acc, 8);
        STAMP(4);
    }
    make_um();
    bar();
    STAMP(5);
    attention();
    STAMP(25);
    if (UNLIKELY(!ok)) { STAMP(15); ov = unit_exact(lds, hd, qb, nskip, lane); }
    __builtin_amdgcn_raw_buffer_store_b128(sanitize(ov), wsr, oslot, 0, 16);
    STAMP(6);

    {
        asm volatile("s_waitcnt vmcnt(1)" : "+v"(ocf) :: "memory");
        bar();
        flag_poll(ocf, foff_o);
        bf16x8 wa[8][2];
        {
            LAS char* wb2 = lds + (int)((unsigned)(wb - lds) ^ 64u);
#pragma unroll
            for (int j = 0; j < 8; ++j) {
                wa[j][0] = tr2(wb + j * 2048, wb + j * 2048 + 1024);
                wa[j][1] = tr2(wb2 + j * 2048, wb2 + j * 2048 + 1024);
            }
        }
        u32x4 d[8];
        {
            const unsigned pofs = osrc - lane * 16 + (lane & 7) * 1024 + 1020;
            unsigned got = 0;
#pragma unroll 1
            for (int spin = 0; spin < SPIN_CAP; ++spin) {
                CFENCE();
                const unsigned pr = __builtin_amdgcn_raw_buffer_load_b32(wsr, pofs, 0, 16);
                const unsigned rdy = (unsigned)__builtin_amdgcn_ballot_w64(pr != SENT) & 0xFFu;
                const unsigned nw = rdy & ~got;
#pragma unroll
                for (int j = 0; j < 8; ++j)
                    if (nw >> j & 1) d[j] = __builtin_amdgcn_raw_buffer_load_b128(wsr, osrc + j * 1024, 0, 16);
                got |= rdy;
                if (got == 0xFFu) break;
                if (spin == 0) STAMP(7);
                if (__builtin_popcount(got) >= 8 - DIRECT_TAIL) {
#pragma unroll 1
                    for (; spin < SPIN_CAP; ++spin) {
                        CFENCE();
#pragma unroll
                        for (int j = 0; j < 8; ++j)
                            if (!(got >> j & 1)) d[j] = __builtin_amdgcn_raw_buffer_load_b128(wsr, osrc + j * 1024, 0, 16);
#pragma unroll
                        for (int j = 0; j < 8; ++j)
                            if (!(got >> j & 1)) got |= __all(d[j].w != SENT) ? 1u << j : 0u;
                        if (got == 0xFFu) break;
                    }
                    break;
                }
                __builtin_amdgcn_s_sleep(PROBE_SLEEP);
            }
        }
        f32x16 c0 = {}, c1 = {};
        unsigned mx = 0;
#pragma unroll
        for (int j = 0; j < 8; ++j) {
            c0 = MFMA32(wa[j][0], as_bf(d[j]), c0);
            c1 = MFMA32(wa[j][1], as_bf(d[j]), c1);
            mx = max(mx, d[j].w);
        }
        if (UNLIKELY(!__all(mx != SENT))) {
#pragma unroll 1
            for (int spin = 0; spin < SPIN_CAP; ++spin) {
                bool pend = false;
                CFENCE();
#pragma unroll
                for (int j = 0; j < 8; ++j)
                    if (!__all(d[j].w != SENT)) { d[j] = __builtin_amdgcn_raw_buffer_load_b128(wsr, osrc + j * 1024, 0, 16); pend = true; }
                if (!pend) break;
            }
            c0 = f32x16{}; c1 = f32x16{};
#pragma unroll
            for (int j = 0; j < 8; ++j) {
                c0 = MFMA32(wa[j][0], as_bf(d[j]), c0);
                c1 = MFMA32(wa[j][1], as_bf(d[j]), c1);
            }
        }
        STAMP(8);
        if (UNLIKELY(bid == 0 && wave == 7)) {
#pragma unroll 1
            for (int spin = 0; spin < SPIN_CAP; ++spin) {
                CFENCE();
                bool all = true;
#pragma unroll
                for (int i = 0; i < 8; ++i) {
                    const u32x4 f = __builtin_amdgcn_raw_buffer_load_b128(wsr, WS_FLAG + ((i >> 1) * 64 + lane) * 128 + (i & 1) * 16, 0, 16);
                    all &= f.x == tagv && f.y == tagv && f.z == tagv && f.w == tagv;
                }
                if (__all(all)) break;
            }
            if (lane == 0) __builtin_amdgcn_raw_buffer_store_b32(tagv, wsr, WS_EPOCH, 0, 16);
        }
#pragma unroll
        for (int gq = 0; gq < 4; ++gq) {
            f32x4 a = {c0[4 * gq], c0[4 * gq + 1], c0[4 * gq + 2], c0[4 * gq + 3]};
            f32x4 b = {c1[4 * gq], c1[4 * gq + 1], c1[4 * gq + 2], c1[4 * gq + 3]};
            *(LAS f32x4*)(rpw + gq * 32) = a;
            *(LAS f32x4*)(rpw + 4608 + gq * 32) = b;
        }
        bar();
        STAMP(9);
        {
            f32x4 a = *(LAS f32x4*)(rpr), b = *(LAS f32x4*)(rpr + 16);
#pragma unroll
            for (int s = 1; s < 4; ++s) { a += *(LAS f32x4*)(rpr + s * 9216); b += *(LAS f32x4*)(rpr + s * 9216 + 16); }
            const u32x4 r = {pack2(a[0], a[1]), pack2(a[2], a[3]), pack2(b[0], b[1]), pack2(b[2], b[3])};
            asm volatile("global_store_dwordx4 %0, %1, off sc1\n\ts_nop 1" :: "v"(dst), "v"(r) : "memory");
        }
        STAMP(10); CSTAMP(63); STAMP_FLUSH();
    }
}

extern "C" void kernel_launch(void* const* d_in, const int* in_sizes, int n_in, void* d_out, int out_size, void* d_ws,
                              size_t ws_size, hipStream_t stream) {
    (void)in_sizes; (void)n_in; (void)out_size; (void)ws_size;
    (void)hipFuncSetAttribute((const void*)swa_block, hipFuncAttributeMaxDynamicSharedMemorySize, LDS_BYTES);
    swa_block<<<256, 512, LDS_BYTES, stream>>>((const u16*)d_in[0], (const u16*)d_in[1], (const u16*)d_in[2], (u16*)d_out, (char*)d_ws);
}
```

```cpp
#include <hip/hip_runtime.h>
#include <stdint.h>
#include <type_traits>

typedef short bf16x8 __attribute__((ext_vector_type(8)));
typedef short s16x4 __attribute__((ext_vector_type(4)));
typedef float f32x16 __attribute__((ext_vector_type(16)));
typedef float f32x4 __attribute__((ext_vector_type(4)));
typedef float f32x2 __attribute__((ext_vector_type(2)));
typedef unsigned u32x4 __attribute__((ext_vector_type(4)));
typedef unsigned u32x2 __attribute__((ext_vector_type(2)));
typedef __bf16 bf16x2 __attribute__((ext_vector_type(2)));
typedef unsigned short u16;
typedef unsigned long long u64;
#define LAS __attribute__((address_space(3)))
#define DI __device__ __forceinline__

#define AFLAG_POS 6
#define PROBE_SLEEP 2
#define DIRECT_TAIL 4
#define LOOK 3
#define WO_G 1
#define PRIO_MODE 2
#define KH_AT 13

constexpr unsigned WS_EPOCH = 0;
constexpr unsigned WS_FLAG = 1024;
constexpr unsigned WS_K = 65536;
constexpr unsigned WS_V = WS_K + 32 * 64 * 1024;
constexpr unsigned WS_O = WS_V + 32 * 64 * 1024;
constexpr unsigned WS_END = WS_O + 64 * 32 * 1024;
constexpr int L_RING = 0;
constexpr int L_K = 65536;
constexpr int L_V = L_K + 40960;
constexpr int L_Q = L_V + 40960;
constexpr int L_ONES = L_Q + 8192;
constexpr int L_RED = L_K;
constexpr int LDS_BYTES = L_ONES + 1024;
constexpr int slot_of(int n) { return (n & 3) * 16384; }

constexpr float CEXP = 0.36067376022224085f;
constexpr float HEADROOM = 32.f;
constexpr unsigned SENT = 0xFFFFFFFFu;
constexpr unsigned OOB = 0x40000000u;
constexpr int SPIN_CAP = 1 << 18;

DI unsigned pack2(float a, float b) {
    f32x2 v = {a, b};
    return __builtin_bit_cast(unsigned, __builtin_convertvector(v, bf16x2));
}
DI u32x4 pack8(const f32x16& a, int o) {
    u32x4 r = {pack2(a[o], a[o + 1]), pack2(a[o + 2], a[o + 3]), pack2(a[o + 4], a[o + 5]), pack2(a[o + 6], a[o + 7])};
    return r;
}
DI u32x4 sanitize(u32x4 v) {
    v.w = min(v.w, SENT - 1);
    return v;
}
DI bf16x8 as_bf(u32x4 v) { return __builtin_bit_cast(bf16x8, v); }
DI u32x4 as_u(bf16x8 v) { return __builtin_bit_cast(u32x4, v); }
DI void waitl0() { asm volatile("" ::: "memory"); __builtin_amdgcn_s_waitcnt(0xC07F); asm volatile("" ::: "memory"); }
DI void bar() { waitl0(); __builtin_amdgcn_s_barrier(); asm volatile("" ::: "memory"); }
template <int N> DI void waitv() { asm volatile("s_waitcnt vmcnt(%0)" :: "i"(N) : "memory"); }
#define CFENCE() asm volatile("" ::: "memory")
#define SB() __builtin_amdgcn_sched_barrier(0)
#define PIN(x) asm volatile("" : "+v"(x))
#define MFMA32(a, b, c) __builtin_amdgcn_mfma_f32_32x32x16_bf16((a), (b), (c), 0, 0, 0)
DI s16x4 trread(LAS char* p) { return __builtin_amdgcn_ds_read_tr16_b64_v4i16((LAS s16x4*)p); }
DI bf16x8 tr2(LAS char* p0, LAS char* p1) {
    s16x4 a = trread(p0), b = trread(p1);
    return __builtin_shufflevector(a, b, 0, 1, 2, 3, 4, 5, 6, 7);
}
DI void glds16(const void* sbase, unsigned voff, unsigned ldsaddr) {
    asm volatile("s_mov_b32 m0, %0\n\ts_nop 0\n\tglobal_load_lds_dwordx4 %1, %2" :: "s"(ldsaddr), "v"(voff), "s"(sbase) : "memory");
}
DI void blds16_sc1(__amdgpu_buffer_rsrc_t rs, unsigned voff, unsigned soff, unsigned ldsaddr) {
    asm volatile("s_mov_b32 m0, %0\n\ts_nop 0\n\tbuffer_load_dwordx4 %1, %2, %3 offen sc1 lds" :: "s"(ldsaddr), "v"(voff), "s"(rs), "s"(soff) : "memory");
}
DI float xhalf_max(float m) {
    auto r = __builtin_amdgcn_permlane32_swap(__float_as_uint(m), __float_as_uint(m), false, false);
    return fmaxf(__uint_as_float(r[0]), __uint_as_float(r[1]));
}
DI unsigned min4(u32x4 f) { return __builtin_amdgcn_readfirstlane(min(min(f.x, f.y), min(f.z, f.w))); }
DI u32x4 ldflag4(LAS char* p) { CFENCE(); u32x4 f = *(volatile LAS u32x4*)p; CFENCE(); return f; }
DI unsigned ldflag1(LAS char* p) { CFENCE(); unsigned f = *(volatile LAS unsigned*)p; CFENCE(); return __builtin_amdgcn_readfirstlane(f); }
#define UNLIKELY(c) __builtin_expect(!!(c), 0)
#define AINL __attribute__((always_inline))
template <int V> using IC = std::integral_constant<int, V>;
DI void wait4_ge(LAS char* p, unsigned n) {
    if (UNLIKELY(min4(ldflag4(p)) < n)) {
#pragma unroll 1
        for (int spin = 0; spin < SPIN_CAP; ++spin) if (min4(ldflag4(p)) >= n) break;
    }
}
DI void wait1_ge(LAS char* p, unsigned n) {
    if (UNLIKELY(ldflag1(p) < n)) {
#pragma unroll 1
        for (int spin = 0; spin < SPIN_CAP; ++spin) if (ldflag1(p) >= n) break;
    }
}
DI void blds4k_sc1(__amdgpu_buffer_rsrc_t rs, unsigned voff, unsigned soff, unsigned ldsaddr) {
    asm volatile("s_mov_b32 m0, %0\n\ts_nop 0\n\tbuffer_load_dwordx4 %1, %2, %3 offen sc1 lds\n\t"
                 "buffer_load_dwordx4 %1, %2, %3 offen offset:1024 sc1 lds\n\t"
                 "buffer_load_dwordx4 %1, %2, %3 offen offset:2048 sc1 lds\n\t"
                 "buffer_load_dwordx4 %1, %2, %3 offen offset:3072 sc1 lds" :: "s"(ldsaddr), "v"(voff), "s"(rs), "s"(soff) : "memory");
}
DI void stflag(LAS char* p, unsigned v) { waitl0(); *(volatile LAS unsigned*)p = v; CFENCE(); }
constexpr u64 upper_mask(int i) {
    u64 m = 0;
    for (int l = 0; l < 64; ++l) if ((i & 3) + 8 * (i >> 2) + 4 * (l >> 5) > (l & 31)) m |= 1ull << l;
    return m;
}
DI float sel(float lo, float hi, u64 m) { return __builtin_amdgcn_inverse_ballot_w64(m) ? hi : lo; }

#define STAMP(i) do {} while (0)
#define CSTAMP(i) do {} while (0)
#define STAMP_FLUSH() do {} while (0)

DI u32x4 unit_exact(LAS char* lds, int hd, int qb, int nskip, int lane) {
    const int r32 = lane & 31, hh = lane >> 5;
    const bf16x8 qf = *(LAS bf16x8*)(lds + L_Q + hd * 2048 + qb * 1024 + lane * 16);
    LAS char* kp = lds + L_K + hd * 10240 + qb * 1024 + lane * 16;
    const int va0 = (r32 < 16) ? (L_V + hd * 10240 + qb * 1024 + hh * 256 + r32 * 16) : (L_ONES + qb * 256);
    const int vstr = (r32 < 16) ? 1024 : 16;
    const float NINF = -__builtin_inff();
    const int t0 = nskip > qb ? nskip - qb : 0;
    const f32x16 zero = {};
    float m = NINF;
#pragma unroll 1
    for (int t = t0; t < 9; ++t) {
        f32x16 S = MFMA32(*(LAS bf16x8*)(kp + t * 1024), qf, zero);
#pragma unroll
        for (int i = 0; i < 16; ++i) {
            const int key = (i & 3) + 8 * (i >> 2) + 4 * hh;
            const bool ok = t == 0 ? key > r32 : t == 8 ? key <= r32 : true;
            m = fmaxf(m, ok ? S[i] : NINF);
        }
    }
    m = xhalf_max(m);
    f32x16 O = {};
#pragma unroll 1
    for (int t = t0; t < 9; ++t) {
        f32x16 S = MFMA32(*(LAS bf16x8*)(kp + t * 1024), qf, zero);
#pragma unroll
        for (int i = 0; i < 16; ++i) {
            const int key = (i & 3) + 8 * (i >> 2) + 4 * hh;
            const bool ok = t == 0 ? key > r32 : t == 8 ? key <= r32 : true;
            S[i] = ok ? __builtin_amdgcn_exp2f(S[i] - m) : 0.f;
        }
        LAS char* vp = lds + va0 + t * vstr;
        O = MFMA32(*(LAS bf16x8*)(vp), as_bf(pack8(S, 0)), O);
        O = MFMA32(*(LAS bf16x8*)(vp + 512), as_bf(pack8(S, 8)), O);
    }
    const float rl = __builtin_amdgcn_rcpf(O[8]);
#pragma unroll
    for (int i = 0; i < 8; ++i) O[i] *= rl;
    return pack8(O, 0);
}

__global__ __launch_bounds__(512, 2) void swa_block(const u16* __restrict__ x, const u16* __restrict__ wqkv,
                                                    const u16* __restrict__ wo, u16* __restrict__ out, char* ws) {
    extern __shared__ __attribute__((aligned(16))) char smem_[];
    LAS char* lds = (LAS char*)smem_;
    asm volatile("" :: "s"(x), "s"(wqkv), "s"(wo), "s"(out), "s"(ws));
    const int tid = threadIdx.x;
    const int wave = __builtin_amdgcn_readfirstlane(tid >> 6);
    const int lane = tid & 63, r32 = lane & 31, hh = lane >> 5;
    const int bid = blockIdx.x, g = bid & 7, ii = bid >> 3;
    const int hg = (g & 1) * 4 + (ii & 3);
    const int ck = (g >> 1) * 8 + (ii >> 2);
    const int rot = ((ii >> 2) + 2 * (ii & 3)) & 7;
    const unsigned lbase = (unsigned)(uintptr_t)lds;
    const int hd = wave & 3;
    const bool isL = wave >= 4;
    unsigned stampv = 0; (void)stampv;
    STAMP(0); CSTAMP(62);
    const unsigned wchunk = (unsigned)(((((lane >> 2) & 1) ^ ((lane >> 4) & 1)) * 4 + (lane & 3)) * 16);
    const unsigned xvo = (unsigned)((lane >> 3) * 512 + (((lane & 7) ^ (lane >> 4) ^ ((hd & 1) << 2)) * 8)) * 2u;
    const unsigned wvo = (unsigned)((lane >> 3) * 3072) + wchunk;
    const u16* xsrc = x + (size_t)(ck * 64 + hd * 8) * 512;
    const u16* wsrc = wqkv + (size_t)(hd * 8) * 1536 + hg * 64;
    auto issue_w = [&](int s, int col, unsigned d) AINL {
        const u16* wp = wsrc + (size_t)((s + rot) & 7) * 64 * 1536 + col;
        glds16(wp, wvo, d);
        glds16(wp + 32 * 1536, wvo, d + 4096);
    };
    auto issue_step = [&](int n) AINL {
        const unsigned d = lbase + L_RING + slot_of(n) + hd * 1024;
        if (n < 8) {
            const u16* xp = xsrc + ((n + rot) & 7) * 64;
            glds16(xp, xvo, d);
            glds16(xp + 32 * 512, xvo, d + 4096);
            issue_w(n, 512, d + 8192);
        } else {
            issue_w(2 * (n & 3), n < 12 ? 1024 : 0, d);
            issue_w(2 * (n & 3) + 1, n < 12 ? 1024 : 0, d + 8192);
        }
    };
    if (__builtin_expect(isL, 1)) { issue_step(0); issue_step(1); issue_step(2); if (LOOK == 3) issue_step(3); }
    const int orow = g * 256 + (ii >> 3) * 64, ocol = (ii & 7) * 64;
    const __amdgpu_buffer_rsrc_t wsr = __builtin_amdgcn_make_buffer_rsrc(ws, 0, WS_END, 0x00020000);
    const int hbase = 2 * ck - 8;
    const int nskip = hbase < 0 ? -hbase : 0;
    const int q4 = (lane & 15) >> 2;
    const int qb = isL ? 1 : 0;
    const unsigned myflag = WS_FLAG + (unsigned)(hg * 32 + ck) * 128 + wave * 4;
    const u32x4 sentv = {SENT, SENT, SENT, SENT};
    unsigned tagv;

    u32x4 mk0, mk1;
    u64 um[16];
    unsigned foff_h, foff_o;
    unsigned hvo, oslot, osrc;
    LAS char *kp, *vp0, *qp, *hchk, *wb, *rpw, *rpr;
    int vstr;
    u16* dst;
    auto prepare = [&]() AINL {
#pragma unroll
        for (int j = 0; j < 4; ++j) {
            const int ka = ((2 * j) & 3) + 8 * ((2 * j) >> 2) + 4 * hh;
            mk0[j] = ((ka > r32) ? 0xFFFFu : 0u) | ((ka + 1 > r32) ? 0xFFFF0000u : 0u);
            mk1[j] = ((ka + 16 > r32) ? 0xFFFFu : 0u) | ((ka + 17 > r32) ? 0xFFFF0000u : 0u);
        }
        PIN(mk0); PIN(mk1);
        const int l8 = lane & 7;
        const int pc = ck - 1 - (l8 >> 1);
        foff_h = WS_FLAG + (unsigned)(hg * 32 + (pc < 0 ? ck : pc)) * 128 + ((hd >> 1) * 2 + (l8 & 1)) * 4;
        foff_o = WS_FLAG + (unsigned)((2 * hd + (l8 >> 2)) * 32 + (orow >> 6)) * 128 + (qb * 4 + (l8 & 3)) * 4;
        hvo = (unsigned)((int)WS_K + ((hg * 4 + hd) * 64 + hbase) * 1024 + lane * 16);
        hchk = lds + L_K + hd * 10240 + lane * 16 + 12;
        osrc = WS_O + (unsigned)(((orow >> 5) + qb) * 32 + hd * 8) * 1024 + lane * 16;
        kp = lds + L_K + hd * 10240 + qb * 1024 + lane * 16;
        qp = lds + L_Q + hd * 2048 + qb * 1024 + lane * 16;
        vp0 = lds + ((r32 < 16) ? (L_V + hd * 10240 + qb * 1024 + hh * 256 + r32 * 16) : (L_ONES + qb * 256));
        vstr = (r32 < 16) ? 1024 : 16;
        wb = lds + hd * 16384 + (4 * hh + q4) * 128 + ((lane >> 4) & 1) * 32 + (lane & 3) * 8 + ((q4 >> 1) << 6);
        rpw = lds + L_RED + wave * 9216 + r32 * 144 + hh * 16;
        const int qr = hd * 8 + (lane >> 3), co = lane & 7;
        rpr = lds + L_RED + (wave & 4) * 9216 + (co >> 2) * 4608 + qr * 144 + (co & 3) * 32;
        dst = out + (size_t)(orow + qb * 32 + qr) * 512 + ocol + co * 8;
        PIN(foff_h); PIN(foff_o); PIN(hvo); PIN(hchk); PIN(osrc); PIN(kp); PIN(qp); PIN(vp0); PIN(vstr); PIN(wb); PIN(rpw); PIN(rpr); PIN(dst);
    };
    oslot = WS_O + (unsigned)((ck * 2 + qb) * 32 + hg * 4 + hd) * 1024 + lane * 16;
    auto make_um = [&]() AINL {
#pragma unroll
        for (int i = 0; i < 16; ++i) { um[i] = upper_mask(i); asm volatile("" : "+s"(um[i])); }
    };
    const unsigned hdst = lbase + L_K + hd * 10240;
    auto issue_halo = [&](int v) AINL {
#pragma unroll
        for (int i = 0; i < 8; ++i) blds16_sc1(wsr, hvo, i >= nskip ? i * 1024 + v * (WS_V - WS_K) : OOB, hdst + v * 40960 + i * 1024);
    };
    auto halo_probe = [&](int v) AINL -> bool {
        unsigned mx = 0;
        CFENCE();
#pragma unroll
        for (int t = 0; t < 8; ++t) mx = max(mx, *(LAS unsigned*)(hchk + v * 40960 + t * 1024));
        return __all(mx != SENT);
    };
    auto halo_check = [&](int v) AINL {
        if (UNLIKELY(!halo_probe(v))) {
            STAMP(28);
#pragma unroll 1
            for (int spin = 0; spin < SPIN_CAP; ++spin) {
#pragma unroll 1
                for (int i = 0; i < 8; ++i)
                    blds16_sc1(wsr, hvo, __builtin_amdgcn_readfirstlane(i >= nskip ? i * 1024 + v * (WS_V - WS_K) : OOB), __builtin_amdgcn_readfirstlane(hdst + v * 40960 + i * 1024));
                waitv<0>();
                if (halo_probe(v)) break;
            }
        }
    };
    auto flag_load = [&](unsigned off) AINL -> unsigned {
        unsigned f;
        asm volatile("buffer_load_dword %0, %1, %2, 0 offen sc1" : "=v"(f) : "v"(off), "s"(wsr) : "memory");
        return f;
    };
    auto flag_poll = [&](unsigned f, unsigned off) AINL {
        if (UNLIKELY(!__all(f == tagv))) {
#pragma unroll 1
            for (int spin = 0; spin < SPIN_CAP; ++spin) {
                __builtin_amdgcn_s_sleep(1);
                asm volatile("buffer_load_dword %0, %1, %2, 0 offen sc1\n\ts_waitcnt vmcnt(0)" : "=v"(f) : "v"(off), "s"(wsr) : "memory");
                if (__all(f == tagv)) break;
            }
        }
    };
    unsigned ocf = 0;
    u32x4 ov;
    bool ok;

    auto attention = [&](auto ISL) AINL {
        constexpr bool L = decltype(ISL)::value;
        if constexpr (L) { if (PRIO_MODE >= 1) __builtin_amdgcn_s_setprio(1); }
        u32x4 P[9][2];
        f32x16 cn, T0, T1, Sb, O;
        const f32x16 zero = {};
        const bf16x8 qf = *(LAS bf16x8*)qp;
        auto kread = [&](int t) AINL { return *(LAS bf16x8*)(kp + t * 1024); };
        bf16x8 vf[9][2];
        auto vread = [&](int t) AINL { LAS char* vp = vp0 + t * vstr; vf[t][0] = *(LAS bf16x8*)(vp); vf[t][1] = *(LAS bf16x8*)(vp + 512); };
        auto pvh = [&](int t, int h) AINL { O = MFMA32(vf[t][h], as_bf(P[t][h]), O); };
        const u16* wop = wo + (size_t)(hd * 128) * 512 + ocol;
        const unsigned ovo = (unsigned)((lane >> 3) * 1024) + wchunk;
        auto issue_wo = [&](int i0) AINL {
#pragma unroll
            for (int i = i0; i < i0 + 4; ++i) glds16(wop + (size_t)i * 4096, ovo, lbase + hd * 16384 + i * 1024);
        };
        auto exps = [&](int t, f32x16& T, int a0, int a1, int a2, int a3) AINL {
            const int act[4] = {a0, a1, a2, a3};
#pragma unroll
            for (int c = 0; c < 4; ++c) {
                if (act[c] >= 0) pvh(act[c] >> 1, act[c] & 1);
#pragma unroll
                for (int i = 4 * c; i < 4 * c + 4; ++i) T[i] = __builtin_amdgcn_exp2f(T[i]);
                if (c == 1) P[t][0] = pack8(T, 0);
                if (c == 3) P[t][1] = pack8(T, 8);
                SB();
            }
            PIN(P[t][0]); PIN(P[t][1]);
        };
        {
            const bf16x8 k7 = kread(7);
            f32x16 S = MFMA32(k7, qf, zero);
            float m = fmaxf(S[0], S[1]);
#pragma unroll
            for (int i = 2; i < 16; i += 2) m = fmaxf(fmaxf(m, S[i]), S[i + 1]);
            const float nm = -(xhalf_max(m) + HEADROOM);
#pragma unroll
            for (int i = 0; i < 16; ++i) cn[i] = nm;
            T1 = MFMA32(k7, qf, cn);
        }
        T0 = MFMA32(kread(6), qf, cn);
        SB();
        exps(7, T1, -1, -1, -1, -1);
        T1 = MFMA32(kread(5), qf, cn);
        SB();
        exps(6, T0, -1, -1, -1, -1);
        T0 = MFMA32(kread(4), qf, cn);
        STAMP(13);
        if constexpr (L) {
            waitv<0>();
            __builtin_amdgcn_raw_buffer_store_b32(tagv, wsr, myflag, 0, 16);
            halo_check(1);
        }
        bar();
        STAMP(14);
        O = zero;
        vread(7); vread(6);
        if constexpr (L != (WO_G != 0)) issue_wo(0);
        SB();
        if constexpr (L) { if (PRIO_MODE == 3) __builtin_amdgcn_s_setprio(0); }
        exps(5, T1, -1, 14, 15, 12);
        T1 = MFMA32(kread(3), qf, cn);
        vread(5); vread(4);
        if constexpr (L != (WO_G != 0)) issue_wo(4);
        SB();
        exps(4, T0, 13, -1, 10, 11);
        T0 = MFMA32(kread(2), qf, cn);
        vread(3);
        if constexpr (L != (WO_G != 0)) issue_wo(8);
        SB();
        if constexpr (L) { if (PRIO_MODE == 2) __builtin_amdgcn_s_setprio(0); }
        exps(3, T1, 8, -1, 9, -1);
        T1 = MFMA32(kread(1), qf, cn);
        vread(2);
        if constexpr (L != (WO_G != 0)) issue_wo(12);
        SB();
        exps(2, T0, 6, -1, 7, -1);
        T0 = MFMA32(kread(0), qf, cn);
        Sb = MFMA32(kread(8), qf, cn);
        vread(1);
        ocf = flag_load(foff_o);
        SB();
        exps(1, T1, 4, -1, 5, -1);
        vread(0); vread(8);
        SB();
#pragma unroll
        for (int c = 0; c < 4; ++c) {
            if (c < 2) pvh(1, c);
#pragma unroll
            for (int i = 4 * c; i < 4 * c + 4; ++i) T0[i] = __builtin_amdgcn_exp2f(sel(Sb[i], T0[i], um[i]));
            if (c == 1) { const u32x4 p0 = pack8(T0, 0); P[0][0] = p0 & mk0; P[8][0] = p0 ^ P[0][0]; }
            if (c == 2) { pvh(0, 0); pvh(8, 0); }
            if (c == 3) { const u32x4 p1 = pack8(T0, 8); P[0][1] = p1 & mk1; P[8][1] = p1 ^ P[0][1]; }
            SB();
        }
        pvh(0, 1); pvh(8, 1);
        const float l = O[8];
        const float rl = __builtin_amdgcn_rcpf(l);
        float chk = 0.f;
#pragma unroll
        for (int i = 0; i < 8; ++i) { O[i] *= rl; chk += O[i]; }
        ov = pack8(O, 0);
        ok = __all(l < 0x1p126f && l > 0x1p-100f && __builtin_fabsf(chk) < __builtin_inff());
    };

    if (isL) {
        unsigned tag, kfl = 0;
        asm volatile("s_load_dword %0, %1, 0x0" : "=s"(tag) : "s"(ws) : "memory");
        STAMP(18);
        prepare();
#pragma unroll
        for (int n = 0; n < 16; ++n) {
            constexpr int FL = 12 - LOOK;
            const int last = n + LOOK - (n == 0 ? 0 : 1) < 15 ? n + LOOK - (n == 0 ? 0 : 1) : 15;
            const int young = 4 * (last - n) + ((n > FL && n <= 12) ? 1 : 0) + ((n > KH_AT && n <= (KH_AT + LOOK < 15 ? KH_AT + LOOK : 15)) ? 8 : 0);
            switch (young) {
#define WC(k) case k: waitv<k>(); break;
                WC(0) WC(4) WC(5) WC(8) WC(9) WC(12) WC(13) WC(16) WC(17) WC(20) WC(21)
#undef WC
                default: waitv<0>();
            }
            __builtin_amdgcn_s_barrier();
            if (n == 0) STAMP(20);
            if (n >= 1 && n + LOOK < 16) issue_step(n + LOOK);
            if (n == FL) kfl = flag_load(foff_h);
            if (n == KH_AT) {
                asm volatile("s_waitcnt lgkmcnt(0)" : "+s"(tag), "+v"(kfl) :: "memory");
                tagv = tag + 1u;
                flag_poll(kfl, foff_h);
                issue_halo(0);
            }
        }
        make_um();
        waitv<0>();
        issue_halo(1);
        __builtin_amdgcn_raw_buffer_store_b128(sentv, wsr, oslot, 0, 16);
        halo_check(0);
        STAMP(16);
        __builtin_amdgcn_s_barrier();
        STAMP(5);
        attention(IC<1>{});
    } else {
        const int ft = wave >> 1, tb = wave & 1;
        asm volatile("buffer_load_dword %0, off, %1, 0 sc1" : "=v"(tagv) : "s"(wsr) : "memory");
        const unsigned kb0 = (unsigned)((hg * 4 + 2 * ft) * 64 + ck * 2 + tb) * 1024;
        unsigned kdst = WS_K + kb0 + lane * 16;
        const int vsub = hh * 256 + (r32 & 15) * 16;
        unsigned vdst = WS_V + kb0 + (r32 >> 4) * 65536 + vsub;
        {
            const unsigned o1 = (lane & 15) + ((lane >> 4) & 1) >= nskip ? 0x3F803F80u : 0u;
            const u32x4 one = {o1, o1, o1, o1};
            *(LAS u32x4*)(lds + L_ONES + lane * 16) = one;
        }
        prepare();
        LAS char* xa[4];
        {
            const int u = hh ^ ((r32 >> 1) & 7);
#pragma unroll
            for (int ks = 0; ks < 4; ++ks) { xa[ks] = lds + L_RING + (tb * 32 + r32) * 128 + (((2 * ks) ^ u) << 4); PIN(xa[ks]); }
        }
        LAS char* wa0 = lds + (8 * hh + q4) * 128 + ((ft ^ (q4 >> 1)) << 6) + ((lane >> 4) & 1) * 32 + (lane & 3) * 8;
        LAS char* vl = lds + L_V + (2 * ft + (r32 >> 4)) * 10240 + (8 + tb) * 1024 + vsub;
        LAS char* kl = lds + L_K + (2 * ft) * 10240 + (8 + tb) * 1024 + lane * 16;
        LAS char* ql = lds + L_Q + (2 * ft) * 2048 + tb * 1024 + lane * 16;
        PIN(wa0); PIN(vdst); PIN(kdst); PIN(vl); PIN(kl); PIN(ql);
        auto wrd = [&](int off) AINL { return tr2(wa0 + off, wa0 + off + 512); };
        bf16x8 xf[32], wf[2][8];
        f32x16 acc = {};
#pragma unroll
        for (int n = 0; n <= 16; ++n) {
            const int slot = slot_of(n);
            const int p = n - 1;
            if (n < 16) {
                if (LOOK == 3 && n >= 1 && n + LOOK < 16) waitl0();
                __builtin_amdgcn_s_barrier();
                CFENCE();
                STAMP(30 + n);
            }
            if (p == 8 || p == 12) acc = f32x16{};
            SB();
#pragma unroll
            for (int j = 0; j < 8; ++j) {
                if (p >= 0 && p < 8 && j < 4) acc = MFMA32(wf[p & 1][j], xf[4 * p + j], acc);
                if (p >= 8 && p < 12) acc = MFMA32(xf[8 * (p - 8) + j], wf[p & 1][j], acc);
                if (p >= 12) acc = MFMA32(wf[p & 1][j], xf[8 * (p - 12) + j], acc);
                if (n < 8 && j < 4) {
                    xf[4 * n + j] = *(LAS bf16x8*)(xa[j] + slot);
                    wf[n & 1][j] = wrd(slot + 8192 + j * 2048);
                }
                if (n >= 8 && n < 16 && j < 4) {
                    wf[n & 1][2 * j] = wrd(slot + j * 4096);
                    wf[n & 1][2 * j + 1] = wrd(slot + j * 4096 + 2048);
                }
                SB();
            }
            if (n == 0) {
                __builtin_amdgcn_raw_buffer_store_b128(sentv, wsr, kdst, 0, 16);
                __builtin_amdgcn_raw_buffer_store_b128(sentv, wsr, kdst, 65536, 16);
                __builtin_amdgcn_raw_buffer_store_b128(sentv, wsr, vdst, 0, 16);
                __builtin_amdgcn_raw_buffer_store_b128(sentv, wsr, vdst + 512, 0, 16);
                __builtin_amdgcn_raw_buffer_store_b128(sentv, wsr, oslot, 0, 16);
            }
            if (p == 7) {
                const u32x4 k0 = sanitize(pack8(acc, 0)), k1 = sanitize(pack8(acc, 8));
                __builtin_amdgcn_raw_buffer_store_b128(k0, wsr, kdst, 0, 16);
                __builtin_amdgcn_raw_buffer_store_b128(k1, wsr, kdst, 65536, 16);
                *(LAS u32x4*)(kl) = k0;
                *(LAS u32x4*)(kl + 10240) = k1;
                STAMP(2);
            }
            if (p == 11) {
                const u32x4 v0 = sanitize(pack8(acc, 0)), v1 = sanitize(pack8(acc, 8));
                __builtin_amdgcn_raw_buffer_store_b128(v0, wsr, vdst, 0, 16);
                __builtin_amdgcn_raw_buffer_store_b128(v1, wsr, vdst + 512, 0, 16);
                *(LAS u32x4*)(vl) = v0;
                *(LAS u32x4*)(vl + 512) = v1;
                STAMP(3);
            }
            if (n == AFLAG_POS) {
                asm volatile("s_waitcnt vmcnt(0)" : "+v"(tagv) :: "memory");
                tagv += 1u;
                __builtin_amdgcn_raw_buffer_store_b32(tagv, wsr, myflag, 0, 16);
                STAMP(26);
            }
            if (n == 14) make_um();
        }
#pragma unroll
        for (int i = 0; i < 16; ++i) acc[i] *= CEXP;
        *(LAS u32x4*)(ql) = pack8(acc, 0);
        *(LAS u32x4*)(ql + 2048) = pack8(acc, 8);
        STAMP(4);
        bar();
        STAMP(5);
        attention(IC<0>{});
    }
    __builtin_amdgcn_s_setprio(0);
    STAMP(25);
    if (UNLIKELY(!ok)) { STAMP(15); ov = unit_exact(lds, hd, qb, nskip, lane); }
    __builtin_amdgcn_raw_buffer_store_b128(sanitize(ov), wsr, oslot, 0, 16);
    STAMP(6);

    {
        asm volatile("s_waitcnt vmcnt(1)" : "+v"(ocf) :: "memory");
        bar();
        flag_poll(ocf, foff_o);
        bf16x8 wa[8][2];
        {
            LAS char* wb2 = lds + (int)((unsigned)(wb - lds) ^ 64u);
#pragma unroll
            for (int j = 0; j < 8; ++j) {
                wa[j][0] = tr2(wb + j * 2048, wb + j * 2048 + 1024);
                wa[j][1] = tr2(wb2 + j * 2048, wb2 + j * 2048 + 1024);
            }
        }
        u32x4 d[8];
        {
            const unsigned pofs = osrc - lane * 16 + (lane & 7) * 1024 + 1020;
            unsigned got = 0;
#pragma unroll 1
            for (int spin = 0; spin < SPIN_CAP; ++spin) {
                CFENCE();
                const unsigned pr = __builtin_amdgcn_raw_buffer_load_b32(wsr, pofs, 0, 16);
                const unsigned rdy = (unsigned)__builtin_amdgcn_ballot_w64(pr != SENT) & 0xFFu;
                const unsigned nw = rdy & ~got;
#pragma unroll
                for (int j = 0; j < 8; ++j)
                    if (nw >> j & 1) d[j] = __builtin_amdgcn_raw_buffer_load_b128(wsr, osrc + j * 1024, 0, 16);
                got |= rdy;
                if (got == 0xFFu) break;
                if (spin == 0) STAMP(7);
                if (__builtin_popcount(got) >= 8 - DIRECT_TAIL) {
#pragma unroll 1
                    for (; spin < SPIN_CAP; ++spin) {
                        CFENCE();
#pragma unroll
                        for (int j = 0; j < 8; ++j)
                            if (!(got >> j & 1)) d[j] = __builtin_amdgcn_raw_buffer_load_b128(wsr, osrc + j * 1024, 0, 16);
#pragma unroll
                        for (int j = 0; j < 8; ++j)
                            if (!(got >> j & 1)) got |= __all(d[j].w != SENT) ? 1u << j : 0u;
                        if (got == 0xFFu) break;
                    }
                    break;
                }
                __builtin_amdgcn_s_sleep(PROBE_SLEEP);
            }
        }
        f32x16 c0 = {}, c1 = {};
        unsigned mx = 0;
#pragma unroll
        for (int j = 0; j < 8; ++j) {
            c0 = MFMA32(wa[j][0], as_bf(d[j]), c0);
            c1 = MFMA32(wa[j][1], as_bf(d[j]), c1);
            mx = max(mx, d[j].w);
        }
        if (UNLIKELY(!__all(mx != SENT))) {
#pragma unroll 1
            for (int spin = 0; spin < SPIN_CAP; ++spin) {
                bool pend = false;
                CFENCE();
#pragma unroll
                for (int j = 0; j < 8; ++j)
                    if (!__all(d[j].w != SENT)) { d[j] = __builtin_amdgcn_raw_buffer_load_b128(wsr, osrc + j * 1024, 0, 16); pend = true; }
                if (!pend) break;
            }
            c0 = f32x16{}; c1 = f32x16{};
#pragma unroll
            for (int j = 0; j < 8; ++j) {
                c0 = MFMA32(wa[j][0], as_bf(d[j]), c0);
                c1 = MFMA32(wa[j][1], as_bf(d[j]), c1);
            }
        }
        STAMP(8);
        if (UNLIKELY(bid == 0 && wave == 7)) {
#pragma unroll 1
            for (int spin = 0; spin < SPIN_CAP; ++spin) {
                CFENCE();
                bool all = true;
#pragma unroll
                for (int i = 0; i < 8; ++i) {
                    const u32x4 f = __builtin_amdgcn_raw_buffer_load_b128(wsr, WS_FLAG + ((i >> 1) * 64 + lane) * 128 + (i & 1) * 16, 0, 16);
                    all &= f.x == tagv && f.y == tagv && f.z == tagv && f.w == tagv;
                }
                if (__all(all)) break;
            }
            if (lane == 0) __builtin_amdgcn_raw_buffer_store_b32(tagv, wsr, WS_EPOCH, 0, 16);
        }
#pragma unroll
        for (int gq = 0; gq < 4; ++gq) {
            f32x4 a = {c0[4 * gq], c0[4 * gq + 1], c0[4 * gq + 2], c0[4 * gq + 3]};
            f32x4 b = {c1[4 * gq], c1[4 * gq + 1], c1[4 * gq + 2], c1[4 * gq + 3]};
            *(LAS f32x4*)(rpw + gq * 32) = a;
            *(LAS f32x4*)(rpw + 4608 + gq * 32) = b;
        }
        bar();
        STAMP(9);
        {
            f32x4 a = *(LAS f32x4*)(rpr), b = *(LAS f32x4*)(rpr + 16);
#pragma unroll
            for (int s = 1; s < 4; ++s) { a += *(LAS f32x4*)(rpr + s * 9216); b += *(LAS f32x4*)(rpr + s * 9216 + 16); }
            const u32x4 r = {pack2(a[0], a[1]), pack2(a[2], a[3]), pack2(b[0], b[1]), pack2(b[2], b[3])};
            asm volatile("global_store_dwordx4 %0, %1, off sc1\n\ts_nop 1" :: "v"(dst), "v"(r) : "memory");
        }
        STAMP(10); CSTAMP(63); STAMP_FLUSH();
    }
}

extern "C" void kernel_launch(void* const* d_in, const int* in_sizes, int n_in, void* d_out, int out_size, void* d_ws,
                              size_t ws_size, hipStream_t stream) {
    (void)in_sizes; (void)n_in; (void)out_size; (void)ws_size;
    (void)hipFuncSetAttribute((const void*)swa_block, hipFuncAttributeMaxDynamicSharedMemorySize, LDS_BYTES);
    swa_block<<<256, 512, LDS_BYTES, stream>>>((const u16*)d_in[0], (const u16*)d_in[1], (const u16*)d_in[2], (u16*)d_out, (char*)d_ws);
}
```

```cpp
#define TIMELINE
#define FEWST
#define CLR_AT -1
#define AMPJ 10
#define AMPM 0
#define AMPMODE 0
#include <hip/hip_runtime.h>
#include <stdint.h>
#include <type_traits>

typedef short bf16x8 __attribute__((ext_vector_type(8)));
typedef short s16x4 __attribute__((ext_vector_type(4)));
typedef float f32x16 __attribute__((ext_vector_type(16)));
typedef float f32x4 __attribute__((ext_vector_type(4)));
typedef float f32x2 __attribute__((ext_vector_type(2)));
typedef unsigned u32x4 __attribute__((ext_vector_type(4)));
typedef unsigned u32x2 __attribute__((ext_vector_type(2)));
typedef __bf16 bf16x2 __attribute__((ext_vector_type(2)));
typedef unsigned short u16;
typedef unsigned long long u64;
#define LAS __attribute__((address_space(3)))
#define DI __device__ __forceinline__

#define AFLAG_POS 6
#define PROBE_SLEEP 2
#define DIRECT_TAIL 4
#define LOOK 3
#define WO_G 1
#define KH_AT 13

constexpr unsigned WS_EPOCH = 0;
constexpr unsigned WS_FLAG = 1024;
constexpr unsigned WS_K = 65536;
constexpr unsigned WS_V = WS_K + 32 * 64 * 1024;
constexpr unsigned WS_O = WS_V + 32 * 64 * 1024;
constexpr unsigned WS_END = WS_O + 64 * 32 * 1024;
constexpr unsigned WS_DBG = WS_END;
constexpr int L_RING = 0;
constexpr int L_K = 65536;
constexpr int L_V = L_K + 40960;
constexpr int L_Q = L_V + 40960;
constexpr int L_ONES = L_Q + 8192;
constexpr int L_RED = L_K;
constexpr int LDS_BYTES = L_ONES + 1024;
#define NSLOT 4
constexpr int slot_of(int n) { return (n % NSLOT) * 16384; }

constexpr float CEXP = 0.36067376022224085f;
constexpr float HEADROOM = 32.f;
constexpr unsigned SENT = 0xFFFFFFFFu;
constexpr unsigned OOB = 0x40000000u;
constexpr int SPIN_CAP = 1 << 18;

DI unsigned pack2(float a, float b) {
    f32x2 v = {a, b};
    return __builtin_bit_cast(unsigned, __builtin_convertvector(v, bf16x2));
}
DI u32x4 pack8(const f32x16& a, int o) {
    u32x4 r = {pack2(a[o], a[o + 1]), pack2(a[o + 2], a[o + 3]), pack2(a[o + 4], a[o + 5]), pack2(a[o + 6], a[o + 7])};
    return r;
}
DI u32x4 sanitize(u32x4 v) {
    v.w = min(v.w, SENT - 1);
    return v;
}
DI bf16x8 as_bf(u32x4 v) { return __builtin_bit_cast(bf16x8, v); }
DI u32x4 as_u(bf16x8 v) { return __builtin_bit_cast(u32x4, v); }
DI void waitl0() { asm volatile("" ::: "memory"); __builtin_amdgcn_s_waitcnt(0xC07F); asm volatile("" ::: "memory"); }
DI void bar() { waitl0(); __builtin_amdgcn_s_barrier(); asm volatile("" ::: "memory"); }
template <int N> DI void waitv() { asm volatile("s_waitcnt vmcnt(%0)" :: "i"(N) : "memory"); }
#define CFENCE() asm volatile("" ::: "memory")
#define SB() __builtin_amdgcn_sched_barrier(0)
#define PIN(x) asm volatile("" : "+v"(x))
#define MFMA32(a, b, c) __builtin_amdgcn_mfma_f32_32x32x16_bf16((a), (b), (c), 0, 0, 0)
DI s16x4 trread(LAS char* p) { return __builtin_amdgcn_ds_read_tr16_b64_v4i16((LAS s16x4*)p); }
DI bf16x8 tr2(LAS char* p0, LAS char* p1) {
    s16x4 a = trread(p0), b = trread(p1);
    return __builtin_shufflevector(a, b, 0, 1, 2, 3, 4, 5, 6, 7);
}
DI void glds16(const void* sbase, unsigned voff, unsigned ldsaddr) {
    asm volatile("s_mov_b32 m0, %0\n\ts_nop 0\n\tglobal_load_lds_dwordx4 %1, %2" :: "s"(ldsaddr), "v"(voff), "s"(sbase) : "memory");
}
DI void blds16_sc1(__amdgpu_buffer_rsrc_t rs, unsigned voff, unsigned soff, unsigned ldsaddr) {
    asm volatile("s_mov_b32 m0, %0\n\ts_nop 0\n\tbuffer_load_dwordx4 %1, %2, %3 offen sc1 lds" :: "s"(ldsaddr), "v"(voff), "s"(rs), "s"(soff) : "memory");
}
DI float xhalf_max(float m) {
    auto r = __builtin_amdgcn_permlane32_swap(__float_as_uint(m), __float_as_uint(m), false, false);
    return fmaxf(__uint_as_float(r[0]), __uint_as_float(r[1]));
}
DI unsigned min4(u32x4 f) { return __builtin_amdgcn_readfirstlane(min(min(f.x, f.y), min(f.z, f.w))); }
DI u32x4 ldflag4(LAS char* p) { CFENCE(); u32x4 f = *(volatile LAS u32x4*)p; CFENCE(); return f; }
DI unsigned ldflag1(LAS char* p) { CFENCE(); unsigned f = *(volatile LAS unsigned*)p; CFENCE(); return __builtin_amdgcn_readfirstlane(f); }
#define UNLIKELY(c) __builtin_expect(!!(c), 0)
#define AINL __attribute__((always_inline))
template <int V> using IC = std::integral_constant<int, V>;
DI void wait4_ge(LAS char* p, unsigned n) {
    if (UNLIKELY(min4(ldflag4(p)) < n)) {
#pragma unroll 1
        for (int spin = 0; spin < SPIN_CAP; ++spin) if (min4(ldflag4(p)) >= n) break;
    }
}
DI void wait1_ge(LAS char* p, unsigned n) {
    if (UNLIKELY(ldflag1(p) < n)) {
#pragma unroll 1
        for (int spin = 0; spin < SPIN_CAP; ++spin) if (ldflag1(p) >= n) break;
    }
}
DI void blds4k_sc1(__amdgpu_buffer_rsrc_t rs, unsigned voff, unsigned soff, unsigned ldsaddr) {
    asm volatile("s_mov_b32 m0, %0\n\ts_nop 0\n\tbuffer_load_dwordx4 %1, %2, %3 offen sc1 lds\n\t"
                 "buffer_load_dwordx4 %1, %2, %3 offen offset:1024 sc1 lds\n\t"
                 "buffer_load_dwordx4 %1, %2, %3 offen offset:2048 sc1 lds\n\t"
                 "buffer_load_dwordx4 %1, %2, %3 offen offset:3072 sc1 lds" :: "s"(ldsaddr), "v"(voff), "s"(rs), "s"(soff) : "memory");
}
DI void stflag(LAS char* p, unsigned v) { waitl0(); *(volatile LAS unsigned*)p = v; CFENCE(); }
constexpr u64 upper_mask(int i) {
    u64 m = 0;
    for (int l = 0; l < 64; ++l) if ((i & 3) + 8 * (i >> 2) + 4 * (l >> 5) > (l & 31)) m |= 1ull << l;
    return m;
}
DI float sel(float lo, float hi, u64 m) { return __builtin_amdgcn_inverse_ballot_w64(m) ? hi : lo; }

#define STAMP(i) do { unsigned long long t_ = __builtin_amdgcn_s_memrealtime(); stampv = (lane == (i)) ? (unsigned)t_ : stampv; } while (0)
#define CSTAMP(i) do { unsigned long long t_ = __builtin_amdgcn_s_memtime(); stampv = (lane == (i)) ? (unsigned)t_ : stampv; } while (0)
#define STAMP_FLUSH() do { *(volatile unsigned*)(ws + WS_DBG + ((size_t)(blockIdx.x * 8 + wave) * 64 + lane) * 4) = stampv; } while (0)

DI u32x4 unit_exact(LAS char* lds, int hd, int qb, int nskip, int lane) {
    const int r32 = lane & 31, hh = lane >> 5;
    const bf16x8 qf = *(LAS bf16x8*)(lds + L_Q + hd * 2048 + qb * 1024 + lane * 16);
    LAS char* kp = lds + L_K + hd * 10240 + qb * 1024 + lane * 16;
    const int va0 = (r32 < 16) ? (L_V + hd * 10240 + qb * 1024 + hh * 256 + r32 * 16) : (L_ONES + qb * 256);
    const int vstr = (r32 < 16) ? 1024 : 16;
    const float NINF = -__builtin_inff();
    const int t0 = nskip > qb ? nskip - qb : 0;
    const f32x16 zero = {};
    float m = NINF;
#pragma unroll 1
    for (int t = t0; t < 9; ++t) {
        f32x16 S = MFMA32(*(LAS bf16x8*)(kp + t * 1024), qf, zero);
#pragma unroll
        for (int i = 0; i < 16; ++i) {
            const int key = (i & 3) + 8 * (i >> 2) + 4 * hh;
            const bool ok = t == 0 ? key > r32 : t == 8 ? key <= r32 : true;
            m = fmaxf(m, ok ? S[i] : NINF);
        }
    }
    m = xhalf_max(m);
    f32x16 O = {};
#pragma unroll 1
    for (int t = t0; t < 9; ++t) {
        f32x16 S = MFMA32(*(LAS bf16x8*)(kp + t * 1024), qf, zero);
#pragma unroll
        for (int i = 0; i < 16; ++i) {
            const int key = (i & 3) + 8 * (i >> 2) + 4 * hh;
            const bool ok = t == 0 ? key > r32 : t == 8 ? key <= r32 : true;
            S[i] = ok ? __builtin_amdgcn_exp2f(S[i] - m) : 0.f;
        }
        LAS char* vp = lds + va0 + t * vstr;
        O = MFMA32(*(LAS bf16x8*)(vp), as_bf(pack8(S, 0)), O);
        O = MFMA32(*(LAS bf16x8*)(vp + 512), as_bf(pack8(S, 8)), O);
    }
    const float rl = __builtin_amdgcn_rcpf(O[8]);
#pragma unroll
    for (int i = 0; i < 8; ++i) O[i] *= rl;
    return pack8(O, 0);
}

__global__ __launch_bounds__(512, 2) void swa_block(const u16* __restrict__ x, const u16* __restrict__ wqkv,
                                                    const u16* __restrict__ wo, u16* __restrict__ out, char* ws) {
    extern __shared__ __attribute__((aligned(16))) char smem_[];
    LAS char* lds = (LAS char*)smem_;
    asm volatile("" :: "s"(x), "s"(wqkv), "s"(wo), "s"(out), "s"(ws));
    const int tid = threadIdx.x;
    const int wave = __builtin_amdgcn_readfirstlane(tid >> 6);
    const int lane = tid & 63, r32 = lane & 31, hh = lane >> 5;
    const int bid = blockIdx.x, g = bid & 7, ii = bid >> 3;
    const int hg = (g & 1) * 4 + (ii & 3);
    const int ck = (g >> 1) * 8 + (ii >> 2);
    const int rot = ((ii >> 2) + 2 * (ii & 3)) & 7;
    const unsigned lbase = (unsigned)(uintptr_t)lds;
    const int hd = wave & 3;
    const bool isL = wave >= 4;
    unsigned stampv = 0; (void)stampv;
    STAMP(0); CSTAMP(62);
    const unsigned wchunk = (unsigned)(((((lane >> 2) & 1) ^ ((lane >> 4) & 1)) * 4 + (lane & 3)) * 16);
    const unsigned xvo = (unsigned)((lane >> 3) * 512 + (((lane & 7) ^ (lane >> 4) ^ ((hd & 1) << 2)) * 8)) * 2u;
    const unsigned wvo = (unsigned)((lane >> 3) * 3072) + wchunk;
    const u16* xsrc = x + (size_t)(ck * 64 + hd * 8) * 512;
    const u16* wsrc = wqkv + (size_t)(hd * 8) * 1536 + hg * 64;
    auto issue_w = [&](int s, int col, unsigned d) AINL {
        const u16* wp = wsrc + (size_t)((s + rot) & 7) * 64 * 1536 + col;
        glds16(wp, wvo, d);
        glds16(wp + 32 * 1536, wvo, d + 4096);
    };
    auto issue_step = [&](int n) AINL {
        const unsigned d = lbase + L_RING + slot_of(n) + hd * 1024;
        if (n < 8) {
            const u16* xp = xsrc + ((n + rot) & 7) * 64;
            glds16(xp, xvo, d);
            glds16(xp + 32 * 512, xvo, d + 4096);
            issue_w(n, 512, d + 8192);
        } else {
            issue_w(2 * (n & 3), n < 12 ? 1024 : 0, d);
            issue_w(2 * (n & 3) + 1, n < 12 ? 1024 : 0, d + 8192);
        }
    };
    if (__builtin_expect(isL, 1)) {
#pragma unroll
 for (int i = 0; i <= LOOK; ++i) issue_step(i); }
    const int orow = g * 256 + (ii >> 3) * 64, ocol = (ii & 7) * 64;
    const __amdgpu_buffer_rsrc_t wsr = __builtin_amdgcn_make_buffer_rsrc(ws, 0, WS_END, 0x00020000);
    const int hbase = 2 * ck - 8;
    const int nskip = hbase < 0 ? -hbase : 0;
    const int q4 = (lane & 15) >> 2;
    const int qb = isL ? 1 : 0;
    const unsigned myflag = WS_FLAG + (unsigned)(hg * 32 + ck) * 128 + wave * 4;
    const u32x4 sentv = {SENT, SENT, SENT, SENT};
    unsigned tagv;

    u32x4 mk0, mk1;
    u64 um[16];
    unsigned foff_h, foff_o;
    unsigned hvo, oslot, osrc;
    LAS char *kp, *vp0, *qp, *hchk, *wb, *rpw, *rpr;
    int vstr;
    u16* dst;
    auto prepare = [&]() AINL {
#pragma unroll
        for (int j = 0; j < 4; ++j) {
            const int ka = ((2 * j) & 3) + 8 * ((2 * j) >> 2) + 4 * hh;
            mk0[j] = ((ka > r32) ? 0xFFFFu : 0u) | ((ka + 1 > r32) ? 0xFFFF0000u : 0u);
            mk1[j] = ((ka + 16 > r32) ? 0xFFFFu : 0u) | ((ka + 17 > r32) ? 0xFFFF0000u : 0u);
        }
        PIN(mk0); PIN(mk1);
        const int l8 = lane & 7;
        const int pc = ck - 1 - (l8 >> 1);
        foff_h = WS_FLAG + (unsigned)(hg * 32 + (pc < 0 ? ck : pc)) * 128 + ((hd >> 1) * 2 + (l8 & 1)) * 4;
        foff_o = WS_FLAG + (unsigned)((2 * hd + (l8 >> 2)) * 32 + (orow >> 6)) * 128 + (qb * 4 + (l8 & 3)) * 4;
        hvo = (unsigned)((int)WS_K + ((hg * 4 + hd) * 64 + hbase) * 1024 + lane * 16);
        hchk = lds + L_K + hd * 10240 + lane * 16 + 12;
        osrc = WS_O + (unsigned)(((orow >> 5) + qb) * 32 + hd * 8) * 1024 + lane * 16;
        kp = lds + L_K + hd * 10240 + qb * 1024 + lane * 16;
        qp = lds + L_Q + hd * 2048 + qb * 1024 + lane * 16;
        vp0 = lds + ((r32 < 16) ? (L_V + hd * 10240 + qb * 1024 + hh * 256 + r32 * 16) : (L_ONES + qb * 256));
        vstr = (r32 < 16) ? 1024 : 16;
        wb = lds + hd * 16384 + (4 * hh + q4) * 128 + ((lane >> 4) & 1) * 32 + (lane & 3) * 8 + ((q4 >> 1) << 6);
        rpw = lds + L_RED + wave * 9216 + r32 * 144 + hh * 16;
        const int qr = hd * 8 + (lane >> 3), co = lane & 7;
        rpr = lds + L_RED + (wave & 4) * 9216 + (co >> 2) * 4608 + qr * 144 + (co & 3) * 32;
        dst = out + (size_t)(orow + qb * 32 + qr) * 512 + ocol + co * 8;
        PIN(foff_h); PIN(foff_o); PIN(hvo); PIN(hchk); PIN(osrc); PIN(kp); PIN(qp); PIN(vp0); PIN(vstr); PIN(wb); PIN(rpw); PIN(rpr); PIN(dst);
    };
    oslot = WS_O + (unsigned)((ck * 2 + qb) * 32 + hg * 4 + hd) * 1024 + lane * 16;
    auto make_um = [&]() AINL {
#pragma unroll
        for (int i = 0; i < 16; ++i) { um[i] = upper_mask(i); asm volatile("" : "+s"(um[i])); }
    };
    const unsigned hdst = lbase + L_K + hd * 10240;
    auto issue_halo = [&](int v) AINL {
#pragma unroll
        for (int i = 0; i < 8; ++i) blds16_sc1(wsr, hvo, i >= nskip ? i * 1024 + v * (WS_V - WS_K) : OOB, hdst + v * 40960 + i * 1024);
    };
    auto halo_probe = [&](int v) AINL -> bool {
        unsigned mx = 0;
        CFENCE();
#pragma unroll
        for (int t = 0; t < 8; ++t) mx = max(mx, *(LAS unsigned*)(hchk + v * 40960 + t * 1024));
        return __all(mx != SENT);
    };
    auto halo_check = [&](int v) AINL {
        if (UNLIKELY(!halo_probe(v))) {
            STAMP(28);
#pragma unroll 1
            for (int spin = 0; spin < SPIN_CAP; ++spin) {
#pragma unroll 1
                for (int i = 0; i < 8; ++i)
                    blds16_sc1(wsr, hvo, __builtin_amdgcn_readfirstlane(i >= nskip ? i * 1024 + v * (WS_V - WS_K) : OOB), __builtin_amdgcn_readfirstlane(hdst + v * 40960 + i * 1024));
                waitv<0>();
                if (halo_probe(v)) break;
            }
        }
    };
    auto flag_load = [&](unsigned off) AINL -> unsigned {
        unsigned f;
        asm volatile("buffer_load_dword %0, %1, %2, 0 offen sc1" : "=v"(f) : "v"(off), "s"(wsr) : "memory");
        return f;
    };
    auto flag_poll = [&](unsigned f, unsigned off) AINL {
        if (UNLIKELY(!__all(f == tagv))) {
#pragma unroll 1
            for (int spin = 0; spin < SPIN_CAP; ++spin) {
                __builtin_amdgcn_s_sleep(1);
                asm volatile("buffer_load_dword %0, %1, %2, 0 offen sc1\n\ts_waitcnt vmcnt(0)" : "=v"(f) : "v"(off), "s"(wsr) : "memory");
                if (__all(f == tagv)) break;
            }
        }
    };
    unsigned ocf = 0;
    u32x4 ov;
    bool ok;

    auto attention = [&](auto ISL) AINL {
        constexpr bool L = decltype(ISL)::value;
        u32x4 P[9][2];
        f32x16 cn, T0, T1, Sb, O;
        const f32x16 zero = {};
        const bf16x8 qf = *(LAS bf16x8*)qp;
        auto kread = [&](int t) AINL { return *(LAS bf16x8*)(kp + t * 1024); };
        bf16x8 vf[9][2];
        auto vread = [&](int t) AINL { LAS char* vp = vp0 + t * vstr; vf[t][0] = *(LAS bf16x8*)(vp); vf[t][1] = *(LAS bf16x8*)(vp + 512); };
        auto pvh = [&](int t, int h) AINL { O = MFMA32(vf[t][h], as_bf(P[t][h]), O); };
        const u16* wop = wo + (size_t)(hd * 128) * 512 + ocol;
        const unsigned ovo = (unsigned)((lane >> 3) * 1024) + wchunk;
        auto issue_wo = [&](int i0) AINL {
#pragma unroll
            for (int i = i0; i < i0 + 4; ++i) glds16(wop + (size_t)i * 4096, ovo, lbase + hd * 16384 + i * 1024);
        };
        auto exps = [&](int t, f32x16& T, int a0, int a1, int a2, int a3) AINL {
            const int act[4] = {a0, a1, a2, a3};
#pragma unroll
            for (int c = 0; c < 4; ++c) {
                if (act[c] >= 0) pvh(act[c] >> 1, act[c] & 1);
#pragma unroll
                for (int i = 4 * c; i < 4 * c + 4; ++i) T[i] = __builtin_amdgcn_exp2f(T[i]);
                if (c == 1) P[t][0] = pack8(T, 0);
                if (c == 3) P[t][1] = pack8(T, 8);
                SB();
            }
            PIN(P[t][0]); PIN(P[t][1]);
        };
        {
            const bf16x8 k7 = kread(7);
            f32x16 S = MFMA32(k7, qf, zero);
            float m = fmaxf(S[0], S[1]);
#pragma unroll
            for (int i = 2; i < 16; i += 2) m = fmaxf(fmaxf(m, S[i]), S[i + 1]);
            const float nm = -(xhalf_max(m) + HEADROOM);
#pragma unroll
            for (int i = 0; i < 16; ++i) cn[i] = nm;
            T1 = MFMA32(k7, qf, cn);
        }
        T0 = MFMA32(kread(6), qf, cn);
        SB();
        exps(7, T1, -1, -1, -1, -1);
        T1 = MFMA32(kread(5), qf, cn);
        SB();
        exps(6, T0, -1, -1, -1, -1);
        T0 = MFMA32(kread(4), qf, cn);
        STAMP(13);
        if constexpr (L) {
            waitv<0>();
            __builtin_amdgcn_raw_buffer_store_b32(tagv, wsr, myflag, 0, 16);
            halo_check(1);
        }
        bar();
        STAMP(14);
        O = zero;
        vread(7); vread(6);
        if constexpr (L != (WO_G != 0)) issue_wo(0);
        SB();
        exps(5, T1, -1, 14, 15, 12);
        T1 = MFMA32(kread(3), qf, cn);
        vread(5); vread(4);
        if constexpr (L != (WO_G != 0)) issue_wo(4);
        SB();
        exps(4, T0, 13, -1, 10, 11);
        T0 = MFMA32(kread(2), qf, cn);
        vread(3);
        if constexpr (L != (WO_G != 0)) issue_wo(8);
        SB();
        exps(3, T1, 8, -1, 9, -1);
        T1 = MFMA32(kread(1), qf, cn);
        vread(2);
        if constexpr (L != (WO_G != 0)) issue_wo(12);
        SB();
        exps(2, T0, 6, -1, 7, -1);
        T0 = MFMA32(kread(0), qf, cn);
        Sb = MFMA32(kread(8), qf, cn);
        vread(1);
        ocf = flag_load(foff_o);
        SB();
        exps(1, T1, 4, -1, 5, -1);
        vread(0); vread(8);
        SB();
#pragma unroll
        for (int c = 0; c < 4; ++c) {
            if (c < 2) pvh(1, c);
#pragma unroll
            for (int i = 4 * c; i < 4 * c + 4; ++i) T0[i] = __builtin_amdgcn_exp2f(sel(Sb[i], T0[i], um[i]));
            if (c == 1) { const u32x4 p0 = pack8(T0, 0); P[0][0] = p0 & mk0; P[8][0] = p0 ^ P[0][0]; }
            if (c == 2) { pvh(0, 0); pvh(8, 0); }
            if (c == 3) { const u32x4 p1 = pack8(T0, 8); P[0][1] = p1 & mk1; P[8][1] = p1 ^ P[0][1]; }
            SB();
        }
        pvh(0, 1); pvh(8, 1);
        const float l = O[8];
        const float rl = __builtin_amdgcn_rcpf(l);
        float chk = 0.f;
#pragma unroll
        for (int i = 0; i < 8; ++i) { O[i] *= rl; chk += O[i]; }
        ov = pack8(O, 0);
        ok = __all(l < 0x1p126f && l > 0x1p-100f && __builtin_fabsf(chk) < __builtin_inff());
    };

    if (isL) {
        unsigned tag, kfl = 0;
        asm volatile("s_load_dword %0, %1, 0x0" : "=s"(tag) : "s"(ws) : "memory");
        STAMP(18);
        prepare();
#pragma unroll
        for (int n = 0; n < 16; ++n) {
            constexpr int FL = 12 - LOOK;
            const int last = n + LOOK - (n == 0 ? 0 : 1) < 15 ? n + LOOK - (n == 0 ? 0 : 1) : 15;
            const int young = 4 * (last - n) + ((n > FL && n <= 12) ? 1 : 0) + ((n > KH_AT && n <= (KH_AT + LOOK < 15 ? KH_AT + LOOK : 15)) ? 8 : 0);
            switch (young) {
#define WC(k) case k: waitv<k>(); break;
                WC(0) WC(1) WC(2) WC(3) WC(4) WC(5) WC(6) WC(7) WC(8) WC(9) WC(10) WC(11) WC(12) WC(13) WC(14) WC(15) WC(16) WC(17) WC(18) WC(19) WC(20) WC(21) WC(22) WC(23) WC(24) WC(25) WC(26) WC(27) WC(28) WC(29) WC(30) WC(31) WC(32) WC(33) WC(34) WC(35) WC(36) WC(37) WC(38) WC(39) WC(40) WC(41) WC(42) WC(43) WC(44)
#undef WC
                default: waitv<0>();
            }
            __builtin_amdgcn_s_barrier();
            if (n == 0) STAMP(20);
            if (n >= 1 && n + LOOK < 16) issue_step(n + LOOK);
            if (n == FL) kfl = flag_load(foff_h);
            if (n == KH_AT) {
                asm volatile("s_waitcnt lgkmcnt(0)" : "+s"(tag), "+v"(kfl) :: "memory");
                tagv = tag + 1u;
                flag_poll(kfl, foff_h);
                issue_halo(0);
            }
        }
        make_um();
        waitv<0>();
        issue_halo(1);
        __builtin_amdgcn_raw_buffer_store_b128(sentv, wsr, oslot, 0, 16);
        halo_check(0);
        STAMP(16);
        __builtin_amdgcn_s_barrier();
        STAMP(5);
        attention(IC<1>{});
    } else {
        const int ft = wave >> 1, tb = wave & 1;
        asm volatile("buffer_load_dword %0, off, %1, 0 sc1" : "=v"(tagv) : "s"(wsr) : "memory");
        const unsigned kb0 = (unsigned)((hg * 4 + 2 * ft) * 64 + ck * 2 + tb) * 1024;
        unsigned kdst = WS_K + kb0 + lane * 16;
        const int vsub = hh * 256 + (r32 & 15) * 16;
        unsigned vdst = WS_V + kb0 + (r32 >> 4) * 65536 + vsub;
        {
            const unsigned o1 = (lane & 15) + ((lane >> 4) & 1) >= nskip ? 0x3F803F80u : 0u;
            const u32x4 one = {o1, o1, o1, o1};
            *(LAS u32x4*)(lds + L_ONES + lane * 16) = one;
        }
        prepare();
        LAS char* xa[4];
        {
            const int u = hh ^ ((r32 >> 1) & 7);
#pragma unroll
            for (int ks = 0; ks < 4; ++ks) { xa[ks] = lds + L_RING + (tb * 32 + r32) * 128 + (((2 * ks) ^ u) << 4); PIN(xa[ks]); }
        }
        LAS char* wa0 = lds + (8 * hh + q4) * 128 + ((ft ^ (q4 >> 1)) << 6) + ((lane >> 4) & 1) * 32 + (lane & 3) * 8;
        LAS char* vl = lds + L_V + (2 * ft + (r32 >> 4)) * 10240 + (8 + tb) * 1024 + vsub;
        LAS char* kl = lds + L_K + (2 * ft) * 10240 + (8 + tb) * 1024 + lane * 16;
        LAS char* ql = lds + L_Q + (2 * ft) * 2048 + tb * 1024 + lane * 16;
        PIN(wa0); PIN(vdst); PIN(kdst); PIN(vl); PIN(kl); PIN(ql);
        auto wrd = [&](int off) AINL { return tr2(wa0 + off, wa0 + off + 512); };
        auto clears = [&]() AINL {
                __builtin_amdgcn_raw_buffer_store_b128(sentv, wsr, kdst, 0, 16);
                __builtin_amdgcn_raw_buffer_store_b128(sentv, wsr, kdst, 65536, 16);
                __builtin_amdgcn_raw_buffer_store_b128(sentv, wsr, vdst, 0, 16);
                __builtin_amdgcn_raw_buffer_store_b128(sentv, wsr, vdst + 512, 0, 16);
                __builtin_amdgcn_raw_buffer_store_b128(sentv, wsr, oslot, 0, 16);
        };
        if (CLR_AT == -1) {
            clears(); SB(); }
        bf16x8 xf[32], wf[2][8];
        f32x16 acc = {};
#pragma unroll
        for (int n = 0; n <= 16; ++n) {
            const int slot = slot_of(n);
            const int p = n - 1;
            if (n < 16) {
                if (LOOK == NSLOT - 1 && n >= 1 && n + LOOK < 16) waitl0();
                __builtin_amdgcn_s_barrier();
                CFENCE();
                if (n == 0 || n == 4 || n == 8 || n == 12) STAMP(30 + n);
            }
            if (p == 8 || p == 12) acc = f32x16{};
            SB();
#pragma unroll
            for (int j = 0; j < 8; ++j) {
                if (p >= 0 && p < 8 && j < 4) acc = MFMA32(wf[p & 1][j], xf[4 * p + j], acc);
                if (p >= 8 && p < 12) acc = MFMA32(xf[8 * (p - 8) + j], wf[p & 1][j], acc);
                if (p >= 12) acc = MFMA32(wf[p & 1][j], xf[8 * (p - 12) + j], acc);
                if (n < 8 && j < 4) {
                    xf[4 * n + j] = *(LAS bf16x8*)(xa[j] + slot);
                    wf[n & 1][j] = wrd(slot + 8192 + j * 2048);
                }
                if (n >= 8 && n < 16 && j < 4) {
                    wf[n & 1][2 * j] = wrd(slot + j * 4096);
                    wf[n & 1][2 * j + 1] = wrd(slot + j * 4096 + 2048);
                }
                SB();
            }
            if (n == CLR_AT) clears();
            if (p == 7) {
                const u32x4 k0 = sanitize(pack8(acc, 0)), k1 = sanitize(pack8(acc, 8));
                __builtin_amdgcn_raw_buffer_store_b128(k0, wsr, kdst, 0, 16);
                __builtin_amdgcn_raw_buffer_store_b128(k1, wsr, kdst, 65536, 16);
                *(LAS u32x4*)(kl) = k0;
                *(LAS u32x4*)(kl + 10240) = k1;
                STAMP(2);
            }
            if (p == 11) {
                const u32x4 v0 = sanitize(pack8(acc, 0)), v1 = sanitize(pack8(acc, 8));
                __builtin_amdgcn_raw_buffer_store_b128(v0, wsr, vdst, 0, 16);
                __builtin_amdgcn_raw_buffer_store_b128(v1, wsr, vdst + 512, 0, 16);
                *(LAS u32x4*)(vl) = v0;
                *(LAS u32x4*)(vl + 512) = v1;
                STAMP(3);
            }
            if (n == AFLAG_POS) {
                asm volatile("s_waitcnt vmcnt(0)" : "+v"(tagv) :: "memory");
                tagv += 1u;
                __builtin_amdgcn_raw_buffer_store_b32(tagv, wsr, myflag, 0, 16);
                STAMP(26);
            }
            if (n == 14) make_um();
        }
#pragma unroll
        for (int i = 0; i < 16; ++i) acc[i] *= CEXP;
        *(LAS u32x4*)(ql) = pack8(acc, 0);
        *(LAS u32x4*)(ql + 2048) = pack8(acc, 8);
        STAMP(4);
        bar();
        STAMP(5);
        attention(IC<0>{});
    }
    STAMP(25);
    if (UNLIKELY(!ok)) { STAMP(15); ov = unit_exact(lds, hd, qb, nskip, lane); }
    __builtin_amdgcn_raw_buffer_store_b128(sanitize(ov), wsr, oslot, 0, 16);
    STAMP(6);

    {
        asm volatile("s_waitcnt vmcnt(1)" : "+v"(ocf) :: "memory");
        bar();
        flag_poll(ocf, foff_o);
        bf16x8 wa[8][2];
        {
            LAS char* wb2 = lds + (int)((unsigned)(wb - lds) ^ 64u);
#pragma unroll
            for (int j = 0; j < 8; ++j) {
                wa[j][0] = tr2(wb + j * 2048, wb + j * 2048 + 1024);
                wa[j][1] = tr2(wb2 + j * 2048, wb2 + j * 2048 + 1024);
            }
        }
        u32x4 d[8];
        {
            const unsigned pofs = osrc - lane * 16 + (lane & 7) * 1024 + 1020;
            unsigned got = 0;
#pragma unroll 1
            for (int spin = 0; spin < SPIN_CAP; ++spin) {
                CFENCE();
                const unsigned pr = __builtin_amdgcn_raw_buffer_load_b32(wsr, pofs, 0, 16);
                const unsigned rdy = (unsigned)__builtin_amdgcn_ballot_w64(pr != SENT) & 0xFFu;
                const unsigned nw = rdy & ~got;
#pragma unroll
                for (int j = 0; j < 8; ++j)
                    if (nw >> j & 1) d[j] = __builtin_amdgcn_raw_buffer_load_b128(wsr, osrc + j * 1024, 0, 16);
                got |= rdy;
                if (got == 0xFFu) break;
                if (spin == 0) STAMP(7);
                if (__builtin_popcount(got) >= 8 - DIRECT_TAIL) {
#pragma unroll 1
                    for (; spin < SPIN_CAP; ++spin) {
                        CFENCE();
#pragma unroll
                        for (int j = 0; j < 8; ++j)
                            if (!(got >> j & 1)) d[j] = __builtin_amdgcn_raw_buffer_load_b128(wsr, osrc + j * 1024, 0, 16);
#pragma unroll
                        for (int j = 0; j < 8; ++j)
                            if (!(got >> j & 1)) got |= __all(d[j].w != SENT) ? 1u << j : 0u;
                        if (got == 0xFFu) break;
                    }
                    break;
                }
                __builtin_amdgcn_s_sleep(PROBE_SLEEP);
            }
        }
        f32x16 c0 = {}, c1 = {};
        unsigned mx = 0;
#pragma unroll
        for (int j = 0; j < 8; ++j) {
            c0 = MFMA32(wa[j][0], as_bf(d[j]), c0);
            c1 = MFMA32(wa[j][1], as_bf(d[j]), c1);
            mx = max(mx, d[j].w);
        }
        if (UNLIKELY(!__all(mx != SENT))) {
#pragma unroll 1
            for (int spin = 0; spin < SPIN_CAP; ++spin) {
                bool pend = false;
                CFENCE();
#pragma unroll
                for (int j = 0; j < 8; ++j)
                    if (!__all(d[j].w != SENT)) { d[j] = __builtin_amdgcn_raw_buffer_load_b128(wsr, osrc + j * 1024, 0, 16); pend = true; }
                if (!pend) break;
            }
            c0 = f32x16{}; c1 = f32x16{};
#pragma unroll
            for (int j = 0; j < 8; ++j) {
                c0 = MFMA32(wa[j][0], as_bf(d[j]), c0);
                c1 = MFMA32(wa[j][1], as_bf(d[j]), c1);
            }
        }
        STAMP(8);
        if (UNLIKELY(bid == 0 && wave == 7)) {
#pragma unroll 1
            for (int spin = 0; spin < SPIN_CAP; ++spin) {
                CFENCE();
                bool all = true;
#pragma unroll
                for (int i = 0; i < 8; ++i) {
                    const u32x4 f = __builtin_amdgcn_raw_buffer_load_b128(wsr, WS_FLAG + ((i >> 1) * 64 + lane) * 128 + (i & 1) * 16, 0, 16);
                    all &= f.x == tagv && f.y == tagv && f.z == tagv && f.w == tagv;
                }
                if (__all(all)) break;
            }
            if (lane == 0) __builtin_amdgcn_raw_buffer_store_b32(tagv, wsr, WS_EPOCH, 0, 16);
        }
#pragma unroll
        for (int gq = 0; gq < 4; ++gq) {
            f32x4 a = {c0[4 * gq], c0[4 * gq + 1], c0[4 * gq + 2], c0[4 * gq + 3]};
            f32x4 b = {c1[4 * gq], c1[4 * gq + 1], c1[4 * gq + 2], c1[4 * gq + 3]};
            *(LAS f32x4*)(rpw + gq * 32) = a;
            *(LAS f32x4*)(rpw + 4608 + gq * 32) = b;
        }
        bar();
        STAMP(9);
        {
            f32x4 a = *(LAS f32x4*)(rpr), b = *(LAS f32x4*)(rpr + 16);
#pragma unroll
            for (int s = 1; s < 4; ++s) { a += *(LAS f32x4*)(rpr + s * 9216); b += *(LAS f32x4*)(rpr + s * 9216 + 16); }
            const u32x4 r = {pack2(a[0], a[1]), pack2(a[2], a[3]), pack2(b[0], b[1]), pack2(b[2], b[3])};
            asm volatile("global_store_dwordx4 %0, %1, off sc1\n\ts_nop 1" :: "v"(dst), "v"(r) : "memory");
        }
        STAMP(10); CSTAMP(63); STAMP_FLUSH();
    }
}

__global__ void amp_kernel(char* ws, int j, int mult, int mode) {
    const unsigned* h = (const unsigned*)(ws + WS_DBG);
    const int lane = threadIdx.x;
    unsigned ref = h[0];
    int mn = 0x7fffffff, mx = -0x7fffffff, sum = 0, cnt = 0;
    for (int i = lane; i < 2048; i += 64) {
        int a = (int)(h[i * 64] - ref); mn = min(mn, a);
        unsigned t = h[i * 64 + j]; if (t != 0) { int b = (int)(t - ref); mx = max(mx, b); sum += b; cnt++; }
    }
    for (int o = 32; o; o >>= 1) { mn = min(mn, __shfl_xor(mn, o)); mx = max(mx, __shfl_xor(mx, o)); sum += __shfl_xor(sum, o); cnt += __shfl_xor(cnt, o); }
    int X = (mode ? (sum * 10 / max(cnt, 1)) : mx * 10) - mn * 10; if (X < 0) X = 0; if (X > 100000) X = 100000;
    unsigned long long t0 = __builtin_amdgcn_s_memrealtime();
    while (__builtin_amdgcn_s_memrealtime() - t0 < (unsigned long long)X * mult / 10) __builtin_amdgcn_s_sleep(1);
}

extern "C" void kernel_launch(void* const* d_in, const int* in_sizes, int n_in, void* d_out, int out_size, void* d_ws,
                              size_t ws_size, hipStream_t stream) {
    (void)in_sizes; (void)n_in; (void)out_size; (void)ws_size;
    (void)hipFuncSetAttribute((const void*)swa_block, hipFuncAttributeMaxDynamicSharedMemorySize, LDS_BYTES);
    swa_block<<<256, 512, LDS_BYTES, stream>>>((const u16*)d_in[0], (const u16*)d_in[1], (const u16*)d_in[2], (u16*)d_out, (char*)d_ws);
    amp_kernel<<<1, 64, 0, stream>>>((char*)d_ws, AMPJ, AMPM, AMPMODE);
}
```

```cpp
#define TIMELINE
#define FEWST
#define CLR_AT -1
#define AMPJ 10
#define AMPM 0
#define AMPMODE 0
#include <hip/hip_runtime.h>
#include <stdint.h>
#include <type_traits>

typedef short bf16x8 __attribute__((ext_vector_type(8)));
typedef short s16x4 __attribute__((ext_vector_type(4)));
typedef float f32x16 __attribute__((ext_vector_type(16)));
typedef float f32x4 __attribute__((ext_vector_type(4)));
typedef float f32x2 __attribute__((ext_vector_type(2)));
typedef unsigned u32x4 __attribute__((ext_vector_type(4)));
typedef unsigned u32x2 __attribute__((ext_vector_type(2)));
typedef __bf16 bf16x2 __attribute__((ext_vector_type(2)));
typedef unsigned short u16;
typedef unsigned long long u64;
#define LAS __attribute__((address_space(3)))
#define DI __device__ __forceinline__

#define AFLAG_POS 6
#define PROBE_SLEEP 2
#define DIRECT_TAIL 4
#define LOOK 3
#define WO_G 1
#define KH_AT 13

constexpr unsigned WS_EPOCH = 0;
constexpr unsigned WS_FLAG = 1024;
constexpr unsigned WS_K = 65536;
constexpr unsigned WS_V = WS_K + 32 * 64 * 1024;
constexpr unsigned WS_O = WS_V + 32 * 64 * 1024;
constexpr unsigned WS_END = WS_O + 64 * 32 * 1024;
constexpr unsigned WS_DBG = WS_END;
constexpr int L_RING = 0;
constexpr int L_K = 65536;
constexpr int L_V = L_K + 40960;
constexpr int L_Q = L_V + 40960;
constexpr int L_ONES = L_Q + 8192;
constexpr int L_RED = L_K;
constexpr int LDS_BYTES = L_ONES + 1024;
#define NSLOT 4
constexpr int slot_of(int n) { return (n % NSLOT) * 16384; }

constexpr float CEXP = 0.36067376022224085f;
constexpr float HEADROOM = 32.f;
constexpr unsigned SENT = 0xFFFFFFFFu;
constexpr unsigned OOB = 0x40000000u;
constexpr int SPIN_CAP = 1 << 18;

DI unsigned pack2(float a, float b) {
    f32x2 v = {a, b};
    return __builtin_bit_cast(unsigned, __builtin_convertvector(v, bf16x2));
}
DI u32x4 pack8(const f32x16& a, int o) {
    u32x4 r = {pack2(a[o], a[o + 1]), pack2(a[o + 2], a[o + 3]), pack2(a[o + 4], a[o + 5]), pack2(a[o + 6], a[o + 7])};
    return r;
}
DI u32x4 sanitize(u32x4 v) {
    v.w = min(v.w, SENT - 1);
    return v;
}
DI bf16x8 as_bf(u32x4 v) { return __builtin_bit_cast(bf16x8, v); }
DI u32x4 as_u(bf16x8 v) { return __builtin_bit_cast(u32x4, v); }
DI void waitl0() { asm volatile("" ::: "memory"); __builtin_amdgcn_s_waitcnt(0xC07F); asm volatile("" ::: "memory"); }
DI void bar() { waitl0(); __builtin_amdgcn_s_barrier(); asm volatile("" ::: "memory"); }
template <int N> DI void waitv() { asm volatile("s_waitcnt vmcnt(%0)" :: "i"(N) : "memory"); }
#define CFENCE() asm volatile("" ::: "memory")
#define SB() __builtin_amdgcn_sched_barrier(0)
#define PIN(x) asm volatile("" : "+v"(x))
#define MFMA32(a, b, c) __builtin_amdgcn_mfma_f32_32x32x16_bf16((a), (b), (c), 0, 0, 0)
DI s16x4 trread(LAS char* p) { return __builtin_amdgcn_ds_read_tr16_b64_v4i16((LAS s16x4*)p); }
DI bf16x8 tr2(LAS char* p0, LAS char* p1) {
    s16x4 a = trread(p0), b = trread(p1);
    return __builtin_shufflevector(a, b, 0, 1, 2, 3, 4, 5, 6, 7);
}
DI void glds16(const void* sbase, unsigned voff, unsigned ldsaddr) {
    asm volatile("s_mov_b32 m0, %0\n\ts_nop 0\n\tglobal_load_lds_dwordx4 %1, %2" :: "s"(ldsaddr), "v"(voff), "s"(sbase) : "memory");
}
DI void blds16_sc1(__amdgpu_buffer_rsrc_t rs, unsigned voff, unsigned soff, unsigned ldsaddr) {
    asm volatile("s_mov_b32 m0, %0\n\ts_nop 0\n\tbuffer_load_dwordx4 %1, %2, %3 offen sc1 lds" :: "s"(ldsaddr), "v"(voff), "s"(rs), "s"(soff) : "memory");
}
DI float xhalf_max(float m) {
    auto r = __builtin_amdgcn_permlane32_swap(__float_as_uint(m), __float_as_uint(m), false, false);
    return fmaxf(__uint_as_float(r[0]), __uint_as_float(r[1]));
}
DI unsigned min4(u32x4 f) { return __builtin_amdgcn_readfirstlane(min(min(f.x, f.y), min(f.z, f.w))); }
DI u32x4 ldflag4(LAS char* p) { CFENCE(); u32x4 f = *(volatile LAS u32x4*)p; CFENCE(); return f; }
DI unsigned ldflag1(LAS char* p) { CFENCE(); unsigned f = *(volatile LAS unsigned*)p; CFENCE(); return __builtin_amdgcn_readfirstlane(f); }
#define UNLIKELY(c) __builtin_expect(!!(c), 0)
#define AINL __attribute__((always_inline))
template <int V> using IC = std::integral_constant<int, V>;
DI void wait4_ge(LAS char* p, unsigned n) {
    if (UNLIKELY(min4(ldflag4(p)) < n)) {
#pragma unroll 1
        for (int spin = 0; spin < SPIN_CAP; ++spin) if (min4(ldflag4(p)) >= n) break;
    }
}
DI void wait1_ge(LAS char* p, unsigned n) {
    if (UNLIKELY(ldflag1(p) < n)) {
#pragma unroll 1
        for (int spin = 0; spin < SPIN_CAP; ++spin) if (ldflag1(p) >= n) break;
    }
}
DI void blds4k_sc1(__amdgpu_buffer_rsrc_t rs, unsigned voff, unsigned soff, unsigned ldsaddr) {
    asm volatile("s_mov_b32 m0, %0\n\ts_nop 0\n\tbuffer_load_dwordx4 %1, %2, %3 offen sc1 lds\n\t"
                 "buffer_load_dwordx4 %1, %2, %3 offen offset:1024 sc1 lds\n\t"
                 "buffer_load_dwordx4 %1, %2, %3 offen offset:2048 sc1 lds\n\t"
                 "buffer_load_dwordx4 %1, %2, %3 offen offset:3072 sc1 lds" :: "s"(ldsaddr), "v"(voff), "s"(rs), "s"(soff) : "memory");
}
DI void stflag(LAS char* p, unsigned v) { waitl0(); *(volatile LAS unsigned*)p = v; CFENCE(); }
constexpr u64 upper_mask(int i) {
    u64 m = 0;
    for (int l = 0; l < 64; ++l) if ((i & 3) + 8 * (i >> 2) + 4 * (l >> 5) > (l & 31)) m |= 1ull << l;
    return m;
}
DI float sel(float lo, float hi, u64 m) { return __builtin_amdgcn_inverse_ballot_w64(m) ? hi : lo; }

#define STAMP(i) do { unsigned long long t_ = __builtin_amdgcn_s_memrealtime(); stampv = (lane == (i)) ? (unsigned)t_ : stampv; } while (0)
#define CSTAMP(i) do { unsigned long long t_ = __builtin_amdgcn_s_memtime(); stampv = (lane == (i)) ? (unsigned)t_ : stampv; } while (0)
#define STAMP_FLUSH() do { *(volatile unsigned*)(ws + WS_DBG + ((size_t)(blockIdx.x * 8 + wave) * 64 + lane) * 4) = stampv; } while (0)

DI u32x4 unit_exact(LAS char* lds, int hd, int qb, int nskip, int lane) {
    const int r32 = lane & 31, hh = lane >> 5;
    const bf16x8 qf = *(LAS bf16x8*)(lds + L_Q + hd * 2048 + qb * 1024 + lane * 16);
    LAS char* kp = lds + L_K + hd * 10240 + qb * 1024 + lane * 16;
    const int va0 = (r32 < 16) ? (L_V + hd * 10240 + qb * 1024 + hh * 256 + r32 * 16) : (L_ONES + qb * 256);
    const int vstr = (r32 < 16) ? 1024 : 16;
    const float NINF = -__builtin_inff();
    const int t0 = nskip > qb ? nskip - qb : 0;
    const f32x16 zero = {};
    float m = NINF;
#pragma unroll 1
    for (int t = t0; t < 9; ++t) {
        f32x16 S = MFMA32(*(LAS bf16x8*)(kp + t * 1024), qf, zero);
#pragma unroll
        for (int i = 0; i < 16; ++i) {
            const int key = (i & 3) + 8 * (i >> 2) + 4 * hh;
            const bool ok = t == 0 ? key > r32 : t == 8 ? key <= r32 : true;
            m = fmaxf(m, ok ? S[i] : NINF);
        }
    }
    m = xhalf_max(m);
    f32x16 O = {};
#pragma unroll 1
    for (int t = t0; t < 9; ++t) {
        f32x16 S = MFMA32(*(LAS bf16x8*)(kp + t * 1024), qf, zero);
#pragma unroll
        for (int i = 0; i < 16; ++i) {
            const int key = (i & 3) + 8 * (i >> 2) + 4 * hh;
            const bool ok = t == 0 ? key > r32 : t == 8 ? key <= r32 : true;
            S[i] = ok ? __builtin_amdgcn_exp2f(S[i] - m) : 0.f;
        }
        LAS char* vp = lds + va0 + t * vstr;
        O = MFMA32(*(LAS bf16x8*)(vp), as_bf(pack8(S, 0)), O);
        O = MFMA32(*(LAS bf16x8*)(vp + 512), as_bf(pack8(S, 8)), O);
    }
    const float rl = __builtin_amdgcn_rcpf(O[8]);
#pragma unroll
    for (int i = 0; i < 8; ++i) O[i] *= rl;
    return pack8(O, 0);
}

__global__ __launch_bounds__(512, 2) void swa_block(const u16* __restrict__ x, const u16* __restrict__ wqkv,
                                                    const u16* __restrict__ wo, u16* __restrict__ out, char* ws) {
    extern __shared__ __attribute__((aligned(16))) char smem_[];
    LAS char* lds = (LAS char*)smem_;
    unsigned st0_, st1_, st11_ = 0, st12_ = 0;
    { unsigned long long t1_; asm volatile("s_memrealtime %0" : "=s"(t1_)); st0_ = (unsigned)t1_; }
    asm volatile("" :: "s"(x), "s"(wqkv), "s"(wo), "s"(out), "s"(ws));
    const int tid = threadIdx.x;
    const int wave = __builtin_amdgcn_readfirstlane(tid >> 6);
    const int lane = tid & 63, r32 = lane & 31, hh = lane >> 5;
    const int bid = blockIdx.x, g = bid & 7, ii = bid >> 3;
    const int hg = (g & 1) * 4 + (ii & 3);
    const int ck = (g >> 1) * 8 + (ii >> 2);
    const int rot = ((ii >> 2) + 2 * (ii & 3)) & 7;
    const unsigned lbase = (unsigned)(uintptr_t)lds;
    const int hd = wave & 3;
    const bool isL = wave >= 4;
    unsigned stampv = 0; (void)stampv;
    const unsigned wchunk = (unsigned)(((((lane >> 2) & 1) ^ ((lane >> 4) & 1)) * 4 + (lane & 3)) * 16);
    const unsigned xvo = (unsigned)((lane >> 3) * 512 + (((lane & 7) ^ (lane >> 4) ^ ((hd & 1) << 2)) * 8)) * 2u;
    const unsigned wvo = (unsigned)((lane >> 3) * 3072) + wchunk;
    const u16* xsrc = x + (size_t)(ck * 64 + hd * 8) * 512;
    const u16* wsrc = wqkv + (size_t)(hd * 8) * 1536 + hg * 64;
    auto issue_w = [&](int s, int col, unsigned d) AINL {
        const u16* wp = wsrc + (size_t)((s + rot) & 7) * 64 * 1536 + col;
        glds16(wp, wvo, d);
        glds16(wp + 32 * 1536, wvo, d + 4096);
    };
    auto issue_step = [&](int n) AINL {
        const unsigned d = lbase + L_RING + slot_of(n) + hd * 1024;
        if (n < 8) {
            const u16* xp = xsrc + ((n + rot) & 7) * 64;
            glds16(xp, xvo, d);
            glds16(xp + 32 * 512, xvo, d + 4096);
            issue_w(n, 512, d + 8192);
        } else {
            issue_w(2 * (n & 3), n < 12 ? 1024 : 0, d);
            issue_w(2 * (n & 3) + 1, n < 12 ? 1024 : 0, d + 8192);
        }
    };
    { unsigned long long t1_; asm volatile("s_memrealtime %0" : "=s"(t1_) : "s"(xsrc), "s"(wsrc)); st1_ = (unsigned)t1_; }
    if (__builtin_expect(isL, 1)) {
        issue_step(0);
        { unsigned long long t1_; asm volatile("s_memrealtime %0" : "=s"(t1_)); st11_ = (unsigned)t1_; }
#pragma unroll
        for (int i = 1; i <= LOOK; ++i) issue_step(i);
        { unsigned long long t1_; asm volatile("s_memrealtime %0" : "=s"(t1_)); st12_ = (unsigned)t1_; }
    }
    const int orow = g * 256 + (ii >> 3) * 64, ocol = (ii & 7) * 64;
    const __amdgpu_buffer_rsrc_t wsr = __builtin_amdgcn_make_buffer_rsrc(ws, 0, WS_END, 0x00020000);
    const int hbase = 2 * ck - 8;
    const int nskip = hbase < 0 ? -hbase : 0;
    const int q4 = (lane & 15) >> 2;
    const int qb = isL ? 1 : 0;
    const unsigned myflag = WS_FLAG + (unsigned)(hg * 32 + ck) * 128 + wave * 4;
    const u32x4 sentv = {SENT, SENT, SENT, SENT};
    unsigned tagv;

    u32x4 mk0, mk1;
    u64 um[16];
    unsigned foff_h, foff_o;
    unsigned hvo, oslot, osrc;
    LAS char *kp, *vp0, *qp, *hchk, *wb, *rpw, *rpr;
    int vstr;
    u16* dst;
    auto prepare = [&]() AINL {
#pragma unroll
        for (int j = 0; j < 4; ++j) {
            const int ka = ((2 * j) & 3) + 8 * ((2 * j) >> 2) + 4 * hh;
            mk0[j] = ((ka > r32) ? 0xFFFFu : 0u) | ((ka + 1 > r32) ? 0xFFFF0000u : 0u);
            mk1[j] = ((ka + 16 > r32) ? 0xFFFFu : 0u) | ((ka + 17 > r32) ? 0xFFFF0000u : 0u);
        }
        PIN(mk0); PIN(mk1);
        const int l8 = lane & 7;
        const int pc = ck - 1 - (l8 >> 1);
        foff_h = WS_FLAG + (unsigned)(hg * 32 + (pc < 0 ? ck : pc)) * 128 + ((hd >> 1) * 2 + (l8 & 1)) * 4;
        foff_o = WS_FLAG + (unsigned)((2 * hd + (l8 >> 2)) * 32 + (orow >> 6)) * 128 + (qb * 4 + (l8 & 3)) * 4;
        hvo = (unsigned)((int)WS_K + ((hg * 4 + hd) * 64 + hbase) * 1024 + lane * 16);
        hchk = lds + L_K + hd * 10240 + lane * 16 + 12;
        osrc = WS_O + (unsigned)(((orow >> 5) + qb) * 32 + hd * 8) * 1024 + lane * 16;
        kp = lds + L_K + hd * 10240 + qb * 1024 + lane * 16;
        qp = lds + L_Q + hd * 2048 + qb * 1024 + lane * 16;
        vp0 = lds + ((r32 < 16) ? (L_V + hd * 10240 + qb * 1024 + hh * 256 + r32 * 16) : (L_ONES + qb * 256));
        vstr = (r32 < 16) ? 1024 : 16;
        wb = lds + hd * 16384 + (4 * hh + q4) * 128 + ((lane >> 4) & 1) * 32 + (lane & 3) * 8 + ((q4 >> 1) << 6);
        rpw = lds + L_RED + wave * 9216 + r32 * 144 + hh * 16;
        const int qr = hd * 8 + (lane >> 3), co = lane & 7;
        rpr = lds + L_RED + (wave & 4) * 9216 + (co >> 2) * 4608 + qr * 144 + (co & 3) * 32;
        dst = out + (size_t)(orow + qb * 32 + qr) * 512 + ocol + co * 8;
        PIN(foff_h); PIN(foff_o); PIN(hvo); PIN(hchk); PIN(osrc); PIN(kp); PIN(qp); PIN(vp0); PIN(vstr); PIN(wb); PIN(rpw); PIN(rpr); PIN(dst);
    };
    oslot = WS_O + (unsigned)((ck * 2 + qb) * 32 + hg * 4 + hd) * 1024 + lane * 16;
    auto make_um = [&]() AINL {
#pragma unroll
        for (int i = 0; i < 16; ++i) { um[i] = upper_mask(i); asm volatile("" : "+s"(um[i])); }
    };
    const unsigned hdst = lbase + L_K + hd * 10240;
    auto issue_halo = [&](int v) AINL {
#pragma unroll
        for (int i = 0; i < 8; ++i) blds16_sc1(wsr, hvo, i >= nskip ? i * 1024 + v * (WS_V - WS_K) : OOB, hdst + v * 40960 + i * 1024);
    };
    auto halo_probe = [&](int v) AINL -> bool {
        unsigned mx = 0;
        CFENCE();
#pragma unroll
        for (int t = 0; t < 8; ++t) mx = max(mx, *(LAS unsigned*)(hchk + v * 40960 + t * 1024));
        return __all(mx != SENT);
    };
    auto halo_check = [&](int v) AINL {
        if (UNLIKELY(!halo_probe(v))) {
            STAMP(28);
#pragma unroll 1
            for (int spin = 0; spin < SPIN_CAP; ++spin) {
#pragma unroll 1
                for (int i = 0; i < 8; ++i)
                    blds16_sc1(wsr, hvo, __builtin_amdgcn_readfirstlane(i >= nskip ? i * 1024 + v * (WS_V - WS_K) : OOB), __builtin_amdgcn_readfirstlane(hdst + v * 40960 + i * 1024));
                waitv<0>();
                if (halo_probe(v)) break;
            }
        }
    };
    auto flag_load = [&](unsigned off) AINL -> unsigned {
        unsigned f;
        asm volatile("buffer_load_dword %0, %1, %2, 0 offen sc1" : "=v"(f) : "v"(off), "s"(wsr) : "memory");
        return f;
    };
    auto flag_poll = [&](unsigned f, unsigned off) AINL {
        if (UNLIKELY(!__all(f == tagv))) {
#pragma unroll 1
            for (int spin = 0; spin < SPIN_CAP; ++spin) {
                __builtin_amdgcn_s_sleep(1);
                asm volatile("buffer_load_dword %0, %1, %2, 0 offen sc1\n\ts_waitcnt vmcnt(0)" : "=v"(f) : "v"(off), "s"(wsr) : "memory");
                if (__all(f == tagv)) break;
            }
        }
    };
    unsigned ocf = 0;
    u32x4 ov;
    bool ok;

    auto attention = [&](auto ISL) AINL {
        constexpr bool L = decltype(ISL)::value;
        u32x4 P[9][2];
        f32x16 cn, T0, T1, Sb, O;
        const f32x16 zero = {};
        const bf16x8 qf = *(LAS bf16x8*)qp;
        auto kread = [&](int t) AINL { return *(LAS bf16x8*)(kp + t * 1024); };
        bf16x8 vf[9][2];
        auto vread = [&](int t) AINL { LAS char* vp = vp0 + t * vstr; vf[t][0] = *(LAS bf16x8*)(vp); vf[t][1] = *(LAS bf16x8*)(vp + 512); };
        auto pvh = [&](int t, int h) AINL { O = MFMA32(vf[t][h], as_bf(P[t][h]), O); };
        const u16* wop = wo + (size_t)(hd * 128) * 512 + ocol;
        const unsigned ovo = (unsigned)((lane >> 3) * 1024) + wchunk;
        auto issue_wo = [&](int i0) AINL {
#pragma unroll
            for (int i = i0; i < i0 + 4; ++i) glds16(wop + (size_t)i * 4096, ovo, lbase + hd * 16384 + i * 1024);
        };
        auto exps = [&](int t, f32x16& T, int a0, int a1, int a2, int a3) AINL {
            const int act[4] = {a0, a1, a2, a3};
#pragma unroll
            for (int c = 0; c < 4; ++c) {
                if (act[c] >= 0) pvh(act[c] >> 1, act[c] & 1);
#pragma unroll
                for (int i = 4 * c; i < 4 * c + 4; ++i) T[i] = __builtin_amdgcn_exp2f(T[i]);
                if (c == 1) P[t][0] = pack8(T, 0);
                if (c == 3) P[t][1] = pack8(T, 8);
                SB();
            }
            PIN(P[t][0]); PIN(P[t][1]);
        };
        {
            const bf16x8 k7 = kread(7);
            f32x16 S = MFMA32(k7, qf, zero);
            float m = fmaxf(S[0], S[1]);
#pragma unroll
            for (int i = 2; i < 16; i += 2) m = fmaxf(fmaxf(m, S[i]), S[i + 1]);
            const float nm = -(xhalf_max(m) + HEADROOM);
#pragma unroll
            for (int i = 0; i < 16; ++i) cn[i] = nm;
            T1 = MFMA32(k7, qf, cn);
        }
        T0 = MFMA32(kread(6), qf, cn);
        SB();
        exps(7, T1, -1, -1, -1, -1);
        T1 = MFMA32(kread(5), qf, cn);
        SB();
        exps(6, T0, -1, -1, -1, -1);
        T0 = MFMA32(kread(4), qf, cn);
        STAMP(13);
        if constexpr (L) {
            waitv<0>();
            __builtin_amdgcn_raw_buffer_store_b32(tagv, wsr, myflag, 0, 16);
            halo_check(1);
        }
        bar();
        STAMP(14);
        O = zero;
        vread(7); vread(6);
        if constexpr (L != (WO_G != 0)) issue_wo(0);
        SB();
        exps(5, T1, -1, 14, 15, 12);
        T1 = MFMA32(kread(3), qf, cn);
        vread(5); vread(4);
        if constexpr (L != (WO_G != 0)) issue_wo(4);
        SB();
        exps(4, T0, 13, -1, 10, 11);
        T0 = MFMA32(kread(2), qf, cn);
        vread(3);
        if constexpr (L != (WO_G != 0)) issue_wo(8);
        SB();
        exps(3, T1, 8, -1, 9, -1);
        T1 = MFMA32(kread(1), qf, cn);
        vread(2);
        if constexpr (L != (WO_G != 0)) issue_wo(12);
        SB();
        exps(2, T0, 6, -1, 7, -1);
        T0 = MFMA32(kread(0), qf, cn);
        Sb = MFMA32(kread(8), qf, cn);
        vread(1);
        ocf = flag_load(foff_o);
        SB();
        exps(1, T1, 4, -1, 5, -1);
        vread(0); vread(8);
        SB();
#pragma unroll
        for (int c = 0; c < 4; ++c) {
            if (c < 2) pvh(1, c);
#pragma unroll
            for (int i = 4 * c; i < 4 * c + 4; ++i) T0[i] = __builtin_amdgcn_exp2f(sel(Sb[i], T0[i], um[i]));
            if (c == 1) { const u32x4 p0 = pack8(T0, 0); P[0][0] = p0 & mk0; P[8][0] = p0 ^ P[0][0]; }
            if (c == 2) { pvh(0, 0); pvh(8, 0); }
            if (c == 3) { const u32x4 p1 = pack8(T0, 8); P[0][1] = p1 & mk1; P[8][1] = p1 ^ P[0][1]; }
            SB();
        }
        pvh(0, 1); pvh(8, 1);
        const float l = O[8];
        const float rl = __builtin_amdgcn_rcpf(l);
        float chk = 0.f;
#pragma unroll
        for (int i = 0; i < 8; ++i) { O[i] *= rl; chk += O[i]; }
        ov = pack8(O, 0);
        ok = __all(l < 0x1p126f && l > 0x1p-100f && __builtin_fabsf(chk) < __builtin_inff());
    };

    if (isL) {
        unsigned tag, kfl = 0;
        asm volatile("s_load_dword %0, %1, 0x0" : "=s"(tag) : "s"(ws) : "memory");
        prepare();
#pragma unroll
        for (int n = 0; n < 16; ++n) {
            constexpr int FL = 12 - LOOK;
            const int last = n + LOOK - (n == 0 ? 0 : 1) < 15 ? n + LOOK - (n == 0 ? 0 : 1) : 15;
            const int young = 4 * (last - n) + ((n > FL && n <= 12) ? 1 : 0) + ((n > KH_AT && n <= (KH_AT + LOOK < 15 ? KH_AT + LOOK : 15)) ? 8 : 0);
            switch (young) {
#define WC(k) case k: waitv<k>(); break;
                WC(0) WC(1) WC(2) WC(3) WC(4) WC(5) WC(6) WC(7) WC(8) WC(9) WC(10) WC(11) WC(12) WC(13) WC(14) WC(15) WC(16) WC(17) WC(18) WC(19) WC(20) WC(21) WC(22) WC(23) WC(24) WC(25) WC(26) WC(27) WC(28) WC(29) WC(30) WC(31) WC(32) WC(33) WC(34) WC(35) WC(36) WC(37) WC(38) WC(39) WC(40) WC(41) WC(42) WC(43) WC(44)
#undef WC
                default: waitv<0>();
            }
            if (n == 0) STAMP(17);
            __builtin_amdgcn_s_barrier();
            if (n == 0) STAMP(20);
            if (n >= 1 && n + LOOK < 16) issue_step(n + LOOK);
            if (n == FL) kfl = flag_load(foff_h);
            if (n == KH_AT) {
                asm volatile("s_waitcnt lgkmcnt(0)" : "+s"(tag), "+v"(kfl) :: "memory");
                tagv = tag + 1u;
                flag_poll(kfl, foff_h);
                issue_halo(0);
            }
        }
        make_um();
        waitv<0>();
        issue_halo(1);
        __builtin_amdgcn_raw_buffer_store_b128(sentv, wsr, oslot, 0, 16);
        halo_check(0);
        STAMP(16);
        __builtin_amdgcn_s_barrier();
        STAMP(5);
        attention(IC<1>{});
    } else {
        const int ft = wave >> 1, tb = wave & 1;
        asm volatile("buffer_load_dword %0, off, %1, 0 sc1" : "=v"(tagv) : "s"(wsr) : "memory");
        const unsigned kb0 = (unsigned)((hg * 4 + 2 * ft) * 64 + ck * 2 + tb) * 1024;
        unsigned kdst = WS_K + kb0 + lane * 16;
        const int vsub = hh * 256 + (r32 & 15) * 16;
        unsigned vdst = WS_V + kb0 + (r32 >> 4) * 65536 + vsub;
        {
            const unsigned o1 = (lane & 15) + ((lane >> 4) & 1) >= nskip ? 0x3F803F80u : 0u;
            const u32x4 one = {o1, o1, o1, o1};
            *(LAS u32x4*)(lds + L_ONES + lane * 16) = one;
        }
        prepare();
        LAS char* xa[4];
        {
            const int u = hh ^ ((r32 >> 1) & 7);
#pragma unroll
            for (int ks = 0; ks < 4; ++ks) { xa[ks] = lds + L_RING + (tb * 32 + r32) * 128 + (((2 * ks) ^ u) << 4); PIN(xa[ks]); }
        }
        LAS char* wa0 = lds + (8 * hh + q4) * 128 + ((ft ^ (q4 >> 1)) << 6) + ((lane >> 4) & 1) * 32 + (lane & 3) * 8;
        LAS char* vl = lds + L_V + (2 * ft + (r32 >> 4)) * 10240 + (8 + tb) * 1024 + vsub;
        LAS char* kl = lds + L_K + (2 * ft) * 10240 + (8 + tb) * 1024 + lane * 16;
        LAS char* ql = lds + L_Q + (2 * ft) * 2048 + tb * 1024 + lane * 16;
        PIN(wa0); PIN(vdst); PIN(kdst); PIN(vl); PIN(kl); PIN(ql);
        auto wrd = [&](int off) AINL { return tr2(wa0 + off, wa0 + off + 512); };
        auto clears = [&]() AINL {
                __builtin_amdgcn_raw_buffer_store_b128(sentv, wsr, kdst, 0, 16);
                __builtin_amdgcn_raw_buffer_store_b128(sentv, wsr, kdst, 65536, 16);
                __builtin_amdgcn_raw_buffer_store_b128(sentv, wsr, vdst, 0, 16);
                __builtin_amdgcn_raw_buffer_store_b128(sentv, wsr, vdst + 512, 0, 16);
                __builtin_amdgcn_raw_buffer_store_b128(sentv, wsr, oslot, 0, 16);
        };
        if (CLR_AT == -1) {
            clears(); SB(); }
        STAMP(19);
        bf16x8 xf[32], wf[2][8];
        f32x16 acc = {};
#pragma unroll
        for (int n = 0; n <= 16; ++n) {
            const int slot = slot_of(n);
            const int p = n - 1;
            if (n < 16) {
                if (LOOK == NSLOT - 1 && n >= 1 && n + LOOK < 16) waitl0();
                __builtin_amdgcn_s_barrier();
                CFENCE();
                if (n == 0 || n == 4 || n == 8 || n == 12) STAMP(30 + n);
            }
            if (p == 8 || p == 12) acc = f32x16{};
            SB();
#pragma unroll
            for (int j = 0; j < 8; ++j) {
                if (p >= 0 && p < 8 && j < 4) acc = MFMA32(wf[p & 1][j], xf[4 * p + j], acc);
                if (p >= 8 && p < 12) acc = MFMA32(xf[8 * (p - 8) + j], wf[p & 1][j], acc);
                if (p >= 12) acc = MFMA32(wf[p & 1][j], xf[8 * (p - 12) + j], acc);
                if (n < 8 && j < 4) {
                    xf[4 * n + j] = *(LAS bf16x8*)(xa[j] + slot);
                    wf[n & 1][j] = wrd(slot + 8192 + j * 2048);
                }
                if (n >= 8 && n < 16 && j < 4) {
                    wf[n & 1][2 * j] = wrd(slot + j * 4096);
                    wf[n & 1][2 * j + 1] = wrd(slot + j * 4096 + 2048);
                }
                SB();
            }
            if (n == CLR_AT) clears();
            if (p == 7) {
                const u32x4 k0 = sanitize(pack8(acc, 0)), k1 = sanitize(pack8(acc, 8));
                __builtin_amdgcn_raw_buffer_store_b128(k0, wsr, kdst, 0, 16);
                __builtin_amdgcn_raw_buffer_store_b128(k1, wsr, kdst, 65536, 16);
                *(LAS u32x4*)(kl) = k0;
                *(LAS u32x4*)(kl + 10240) = k1;
                STAMP(2);
            }
            if (p == 11) {
                const u32x4 v0 = sanitize(pack8(acc, 0)), v1 = sanitize(pack8(acc, 8));
                __builtin_amdgcn_raw_buffer_store_b128(v0, wsr, vdst, 0, 16);
                __builtin_amdgcn_raw_buffer_store_b128(v1, wsr, vdst + 512, 0, 16);
                *(LAS u32x4*)(vl) = v0;
                *(LAS u32x4*)(vl + 512) = v1;
                STAMP(3);
            }
            if (n == AFLAG_POS) {
                asm volatile("s_waitcnt vmcnt(0)" : "+v"(tagv) :: "memory");
                tagv += 1u;
                __builtin_amdgcn_raw_buffer_store_b32(tagv, wsr, myflag, 0, 16);
                STAMP(26);
            }
            if (n == 14) make_um();
        }
#pragma unroll
        for (int i = 0; i < 16; ++i) acc[i] *= CEXP;
        *(LAS u32x4*)(ql) = pack8(acc, 0);
        *(LAS u32x4*)(ql + 2048) = pack8(acc, 8);
        STAMP(4);
        bar();
        STAMP(5);
        attention(IC<0>{});
    }
    STAMP(25);
    if (UNLIKELY(!ok)) { STAMP(15); ov = unit_exact(lds, hd, qb, nskip, lane); }
    __builtin_amdgcn_raw_buffer_store_b128(sanitize(ov), wsr, oslot, 0, 16);
    STAMP(6);

    {
        asm volatile("s_waitcnt vmcnt(1)" : "+v"(ocf) :: "memory");
        bar();
        flag_poll(ocf, foff_o);
        bf16x8 wa[8][2];
        {
            LAS char* wb2 = lds + (int)((unsigned)(wb - lds) ^ 64u);
#pragma unroll
            for (int j = 0; j < 8; ++j) {
                wa[j][0] = tr2(wb + j * 2048, wb + j * 2048 + 1024);
                wa[j][1] = tr2(wb2 + j * 2048, wb2 + j * 2048 + 1024);
            }
        }
        u32x4 d[8];
        {
            const unsigned pofs = osrc - lane * 16 + (lane & 7) * 1024 + 1020;
            unsigned got = 0;
#pragma unroll 1
            for (int spin = 0; spin < SPIN_CAP; ++spin) {
                CFENCE();
                const unsigned pr = __builtin_amdgcn_raw_buffer_load_b32(wsr, pofs, 0, 16);
                const unsigned rdy = (unsigned)__builtin_amdgcn_ballot_w64(pr != SENT) & 0xFFu;
                const unsigned nw = rdy & ~got;
#pragma unroll
                for (int j = 0; j < 8; ++j)
                    if (nw >> j & 1) d[j] = __builtin_amdgcn_raw_buffer_load_b128(wsr, osrc + j * 1024, 0, 16);
                got |= rdy;
                if (got == 0xFFu) break;
                if (spin == 0) STAMP(7);
                if (__builtin_popcount(got) >= 8 - DIRECT_TAIL) {
#pragma unroll 1
                    for (; spin < SPIN_CAP; ++spin) {
                        CFENCE();
#pragma unroll
                        for (int j = 0; j < 8; ++j)
                            if (!(got >> j & 1)) d[j] = __builtin_amdgcn_raw_buffer_load_b128(wsr, osrc + j * 1024, 0, 16);
#pragma unroll
                        for (int j = 0; j < 8; ++j)
                            if (!(got >> j & 1)) got |= __all(d[j].w != SENT) ? 1u << j : 0u;
                        if (got == 0xFFu) break;
                    }
                    break;
                }
                __builtin_amdgcn_s_sleep(PROBE_SLEEP);
            }
        }
        f32x16 c0 = {}, c1 = {};
        unsigned mx = 0;
#pragma unroll
        for (int j = 0; j < 8; ++j) {
            c0 = MFMA32(wa[j][0], as_bf(d[j]), c0);
            c1 = MFMA32(wa[j][1], as_bf(d[j]), c1);
            mx = max(mx, d[j].w);
        }
        if (UNLIKELY(!__all(mx != SENT))) {
#pragma unroll 1
            for (int spin = 0; spin < SPIN_CAP; ++spin) {
                bool pend = false;
                CFENCE();
#pragma unroll
                for (int j = 0; j < 8; ++j)
                    if (!__all(d[j].w != SENT)) { d[j] = __builtin_amdgcn_raw_buffer_load_b128(wsr, osrc + j * 1024, 0, 16); pend = true; }
                if (!pend) break;
            }
            c0 = f32x16{}; c1 = f32x16{};
#pragma unroll
            for (int j = 0; j < 8; ++j) {
                c0 = MFMA32(wa[j][0], as_bf(d[j]), c0);
                c1 = MFMA32(wa[j][1], as_bf(d[j]), c1);
            }
        }
        STAMP(8);
        if (UNLIKELY(bid == 0 && wave == 7)) {
#pragma unroll 1
            for (int spin = 0; spin < SPIN_CAP; ++spin) {
                CFENCE();
                bool all = true;
#pragma unroll
                for (int i = 0; i < 8; ++i) {
                    const u32x4 f = __builtin_amdgcn_raw_buffer_load_b128(wsr, WS_FLAG + ((i >> 1) * 64 + lane) * 128 + (i & 1) * 16, 0, 16);
                    all &= f.x == tagv && f.y == tagv && f.z == tagv && f.w == tagv;
                }
                if (__all(all)) break;
            }
            if (lane == 0) __builtin_amdgcn_raw_buffer_store_b32(tagv, wsr, WS_EPOCH, 0, 16);
        }
#pragma unroll
        for (int gq = 0; gq < 4; ++gq) {
            f32x4 a = {c0[4 * gq], c0[4 * gq + 1], c0[4 * gq + 2], c0[4 * gq + 3]};
            f32x4 b = {c1[4 * gq], c1[4 * gq + 1], c1[4 * gq + 2], c1[4 * gq + 3]};
            *(LAS f32x4*)(rpw + gq * 32) = a;
            *(LAS f32x4*)(rpw + 4608 + gq * 32) = b;
        }
        bar();
        STAMP(9);
        {
            f32x4 a = *(LAS f32x4*)(rpr), b = *(LAS f32x4*)(rpr + 16);
#pragma unroll
            for (int s = 1; s < 4; ++s) { a += *(LAS f32x4*)(rpr + s * 9216); b += *(LAS f32x4*)(rpr + s * 9216 + 16); }
            const u32x4 r = {pack2(a[0], a[1]), pack2(a[2], a[3]), pack2(b[0], b[1]), pack2(b[2], b[3])};
            asm volatile("global_store_dwordx4 %0, %1, off sc1\n\ts_nop 1" :: "v"(dst), "v"(r) : "memory");
        }
        asm volatile("s_waitcnt lgkmcnt(0)" : "+s"(st0_), "+s"(st1_), "+s"(st11_), "+s"(st12_));
        stampv = lane == 0 ? st0_ : lane == 1 ? st1_ : lane == 11 ? st11_ : lane == 12 ? st12_ : stampv;
        STAMP(10); STAMP_FLUSH();
    }
}

__global__ void amp_kernel(char* ws, int j, int mult, int mode) {
    const unsigned* h = (const unsigned*)(ws + WS_DBG);
    const int lane = threadIdx.x;
    unsigned ref = h[0];
    int mn = 0x7fffffff, mx = -0x7fffffff, sum = 0, cnt = 0;
    for (int i = lane; i < 2048; i += 64) {
        int a = (int)(h[i * 64] - ref); mn = min(mn, a);
        unsigned t = h[i * 64 + j]; if (t != 0) { int b = (int)(t - ref); mx = max(mx, b); sum += b; cnt++; }
    }
    for (int o = 32; o; o >>= 1) { mn = min(mn, __shfl_xor(mn, o)); mx = max(mx, __shfl_xor(mx, o)); sum += __shfl_xor(sum, o); cnt += __shfl_xor(cnt, o); }
    int X = (mode ? (sum * 10 / max(cnt, 1)) : mx * 10) - mn * 10; if (X < 0) X = 0; if (X > 100000) X = 100000;
    unsigned long long t0 = __builtin_amdgcn_s_memrealtime();
    while (__builtin_amdgcn_s_memrealtime() - t0 < (unsigned long long)X * mult / 10) __builtin_amdgcn_s_sleep(1);
}

extern "C" void kernel_launch(void* const* d_in, const int* in_sizes, int n_in, void* d_out, int out_size, void* d_ws,
                              size_t ws_size, hipStream_t stream) {
    (void)in_sizes; (void)n_in; (void)out_size; (void)ws_size;
    (void)hipFuncSetAttribute((const void*)swa_block, hipFuncAttributeMaxDynamicSharedMemorySize, LDS_BYTES);
    swa_block<<<256, 512, LDS_BYTES, stream>>>((const u16*)d_in[0], (const u16*)d_in[1], (const u16*)d_in[2], (u16*)d_out, (char*)d_ws);
    amp_kernel<<<1, 64, 0, stream>>>((char*)d_ws, AMPJ, AMPM, AMPMODE);
}
```

```cpp
#include <hip/hip_runtime.h>
#include <stdint.h>
#include <type_traits>

typedef short bf16x8 __attribute__((ext_vector_type(8)));
typedef short s16x4 __attribute__((ext_vector_type(4)));
typedef float f32x16 __attribute__((ext_vector_type(16)));
typedef float f32x4 __attribute__((ext_vector_type(4)));
typedef float f32x2 __attribute__((ext_vector_type(2)));
typedef unsigned u32x4 __attribute__((ext_vector_type(4)));
typedef __bf16 bf16x2 __attribute__((ext_vector_type(2)));
typedef unsigned short u16;
typedef unsigned long long u64;
#define LAS __attribute__((address_space(3)))
#define DI __device__ __forceinline__

constexpr int AFLAG_POS = 6;
constexpr int KH_AT = 13;
constexpr int PROBE_SLEEP = 2;
constexpr int DIRECT_TAIL = 4;

constexpr unsigned WS_EPOCH = 0;
constexpr unsigned WS_FLAG = 1024;
constexpr unsigned WS_K = 65536;
constexpr unsigned WS_V = WS_K + 32 * 64 * 1024;
constexpr unsigned WS_O = WS_V + 32 * 64 * 1024;
constexpr unsigned WS_END = WS_O + 64 * 32 * 1024;
constexpr int L_RING = 0;
constexpr int L_K = 65536;
constexpr int L_V = L_K + 40960;
constexpr int L_Q = L_V + 40960;
constexpr int L_ONES = L_Q + 8192;
constexpr int L_RED = L_K;
constexpr int L_MISC = L_ONES + 1024;
constexpr int LDS_BYTES = L_MISC + 256;
constexpr int slot_of(int n) { return (n & 3) * 16384; }

constexpr float CEXP = 0.36067376022224085f;
constexpr float HEADROOM = 32.f;
constexpr unsigned SENT = 0xFFFFFFFFu;
constexpr unsigned OOB = 0x40000000u;
constexpr int SPIN_CAP = 1 << 18;

DI unsigned pack2(float a, float b) {
    f32x2 v = {a, b};
    return __builtin_bit_cast(unsigned, __builtin_convertvector(v, bf16x2));
}
DI u32x4 pack8(const f32x16& a, int o) {
    u32x4 r = {pack2(a[o], a[o + 1]), pack2(a[o + 2], a[o + 3]), pack2(a[o + 4], a[o + 5]), pack2(a[o + 6], a[o + 7])};
    return r;
}
DI u32x4 sanitize(u32x4 v) {
    v.w = min(v.w, SENT - 1);
    return v;
}
DI bf16x8 as_bf(u32x4 v) { return __builtin_bit_cast(bf16x8, v); }
DI void waitl0() { asm volatile("" ::: "memory"); __builtin_amdgcn_s_waitcnt(0xC07F); asm volatile("" ::: "memory"); }
DI void bar() { waitl0(); __builtin_amdgcn_s_barrier(); asm volatile("" ::: "memory"); }
template <int N> DI void waitv() { asm volatile("s_waitcnt vmcnt(%0)" :: "i"(N) : "memory"); }
#define CFENCE() asm volatile("" ::: "memory")
#define SB() __builtin_amdgcn_sched_barrier(0)
#define PIN(x) asm volatile("" : "+v"(x))
#define MFMA32(a, b, c) __builtin_amdgcn_mfma_f32_32x32x16_bf16((a), (b), (c), 0, 0, 0)
DI s16x4 trread(LAS char* p) { return __builtin_amdgcn_ds_read_tr16_b64_v4i16((LAS s16x4*)p); }
DI bf16x8 tr2(LAS char* p0, LAS char* p1) {
    s16x4 a = trread(p0), b = trread(p1);
    return __builtin_shufflevector(a, b, 0, 1, 2, 3, 4, 5, 6, 7);
}
DI void glds16(const void* sbase, unsigned voff, unsigned ldsaddr) {
    asm volatile("s_mov_b32 m0, %0\n\ts_nop 0\n\tglobal_load_lds_dwordx4 %1, %2" :: "s"(ldsaddr), "v"(voff), "s"(sbase) : "memory");
}
DI void blds16_sc1(__amdgpu_buffer_rsrc_t rs, unsigned voff, unsigned soff, unsigned ldsaddr) {
    asm volatile("s_mov_b32 m0, %0\n\ts_nop 0\n\tbuffer_load_dwordx4 %1, %2, %3 offen sc1 lds" :: "s"(ldsaddr), "v"(voff), "s"(rs), "s"(soff) : "memory");
}
DI float xhalf_max(float m) {
    auto r = __builtin_amdgcn_permlane32_swap(__float_as_uint(m), __float_as_uint(m), false, false);
    return fmaxf(__uint_as_float(r[0]), __uint_as_float(r[1]));
}
#define UNLIKELY(c) __builtin_expect(!!(c), 0)
#define INLINE_IF(c) __builtin_expect(!!(c), 1)
#define AINL __attribute__((always_inline))
template <int V> using IC = std::integral_constant<int, V>;
template <int N, int I = 0, class F> DI void static_for(F&& f) { if constexpr (I < N) { f(IC<I>{}); static_for<N, I + 1>(f); } }
constexpr u64 upper_mask(int i) {
    u64 m = 0;
    for (int l = 0; l < 64; ++l) if ((i & 3) + 8 * (i >> 2) + 4 * (l >> 5) > (l & 31)) m |= 1ull << l;
    return m;
}
DI float sel(float lo, float hi, u64 m) { return __builtin_amdgcn_inverse_ballot_w64(m) ? hi : lo; }

#define STAMP(i) do {} while (0)
#define STAMP_FLUSH() do {} while (0)

DI u32x4 unit_exact(LAS char* lds, int hd, int qb, int nskip, int lane) {
    const int r32 = lane & 31, hh = lane >> 5;
    const bf16x8 qf = *(LAS bf16x8*)(lds + L_Q + hd * 2048 + qb * 1024 + lane * 16);
    LAS char* kp = lds + L_K + hd * 10240 + qb * 1024 + lane * 16;
    const int va0 = (r32 < 16) ? (L_V + hd * 10240 + qb * 1024 + hh * 256 + r32 * 16) : (L_ONES + qb * 256);
    const int vstr = (r32 < 16) ? 1024 : 16;
    const float NINF = -__builtin_inff();
    const int t0 = nskip > qb ? nskip - qb : 0;
    const f32x16 zero = {};
    float m = NINF;
#pragma unroll 1
    for (int t = t0; t < 9; ++t) {
        f32x16 S = MFMA32(*(LAS bf16x8*)(kp + t * 1024), qf, zero);
#pragma unroll
        for (int i = 0; i < 16; ++i) {
            const int key = (i & 3) + 8 * (i >> 2) + 4 * hh;
            const bool ok = t == 0 ? key > r32 : t == 8 ? key <= r32 : true;
            m = fmaxf(m, ok ? S[i] : NINF);
        }
    }
    m = xhalf_max(m);
    f32x16 O = {};
#pragma unroll 1
    for (int t = t0; t < 9; ++t) {
        f32x16 S = MFMA32(*(LAS bf16x8*)(kp + t * 1024), qf, zero);
#pragma unroll
        for (int i = 0; i < 16; ++i) {
            const int key = (i & 3) + 8 * (i >> 2) + 4 * hh;
            const bool ok = t == 0 ? key > r32 : t == 8 ? key <= r32 : true;
            S[i] = ok ? __builtin_amdgcn_exp2f(S[i] - m) : 0.f;
        }
        LAS char* vp = lds + va0 + t * vstr;
        O = MFMA32(*(LAS bf16x8*)(vp), as_bf(pack8(S, 0)), O);
        O = MFMA32(*(LAS bf16x8*)(vp + 512), as_bf(pack8(S, 8)), O);
    }
    const float rl = __builtin_amdgcn_rcpf(O[8]);
#pragma unroll
    for (int i = 0; i < 8; ++i) O[i] *= rl;
    return pack8(O, 0);
}

__global__ __launch_bounds__(512, 2) void swa_block(const u16* __restrict__ x, const u16* __restrict__ wqkv,
                                                    const u16* __restrict__ wo, u16* __restrict__ out, char* ws) {
    extern __shared__ __attribute__((aligned(16))) char smem_[];
    LAS char* lds = (LAS char*)smem_;
    {
        unsigned csz;
        asm("s_mov_b32 %0, swa_code_tail-swa_code_head" : "=s"(csz));
        const unsigned off = min((threadIdx.x * 32 + (blockIdx.x >> 3)) * 128u, csz);
        asm volatile("s_getpc_b64 s[90:91]\n"
                     "swa_code_head:\n\t"
                     "v_cmp_gt_u32 vcc, 4, %2\n\t"
                     "s_and_saveexec_b64 s[92:93], vcc\n\t"
                     "s_cbranch_execz 1f\n\t"
                     "s_mov_b32 m0, %0\n\ts_nop 0\n\t"
                     "global_load_lds_dword %1, s[90:91]\n"
                     "1:\n\ts_mov_b64 exec, s[92:93]"
                     :: "s"((unsigned)(uintptr_t)lds + L_MISC), "v"(off), "v"(threadIdx.x) : "memory", "vcc", "scc", "s90", "s91", "s92", "s93");
    }
    SB();
    asm volatile("" :: "s"(x), "s"(wqkv), "s"(wo), "s"(out), "s"(ws));
    const int tid = threadIdx.x;
    const int wave = __builtin_amdgcn_readfirstlane(tid >> 6);
    const int lane = tid & 63, r32 = lane & 31, hh = lane >> 5;
    const int bid = blockIdx.x, g = bid & 7, ii = bid >> 3;
    const int hg = (g & 1) * 4 + (ii & 3);
    const int ck = (g >> 1) * 8 + (ii >> 2);
    const int rot = ((ii >> 2) + 2 * (ii & 3)) & 7;
    const unsigned lbase = (unsigned)(uintptr_t)lds;
    const int hd = wave & 3;
    const bool isL = wave >= 4;
    unsigned stampv = 0; (void)stampv;
    STAMP(0);
    const unsigned wchunk = (unsigned)(((((lane >> 2) & 1) ^ ((lane >> 4) & 1)) * 4 + (lane & 3)) * 16);
    const unsigned xvo = (unsigned)((lane >> 3) * 512 + (((lane & 7) ^ (lane >> 4) ^ ((hd & 1) << 2)) * 8)) * 2u;
    const unsigned wvo = (unsigned)((lane >> 3) * 3072) + wchunk;
    const u16* xsrc = x + (size_t)(ck * 64 + hd * 8) * 512;
    const u16* wsrc = wqkv + (size_t)(hd * 8) * 1536 + hg * 64;
    auto issue_w = [&](int s, int col, unsigned d) AINL {
        const u16* wp = wsrc + (size_t)((s + rot) & 7) * 64 * 1536 + col;
        glds16(wp, wvo, d);
        glds16(wp + 32 * 1536, wvo, d + 4096);
    };
    auto issue_step = [&](int n) AINL {
        const unsigned d = lbase + L_RING + slot_of(n) + hd * 1024;
        if (n < 8) {
            const u16* xp = xsrc + ((n + rot) & 7) * 64;
            glds16(xp, xvo, d);
            glds16(xp + 32 * 512, xvo, d + 4096);
            issue_w(n, 512, d + 8192);
        } else {
            issue_w(2 * (n & 3), n < 12 ? 1024 : 0, d);
            issue_w(2 * (n & 3) + 1, n < 12 ? 1024 : 0, d + 8192);
        }
    };
    if (__builtin_expect(isL, 1)) { issue_step(0); issue_step(1); issue_step(2); issue_step(3); }
    const int orow = g * 256 + (ii >> 3) * 64, ocol = (ii & 7) * 64;
    const __amdgpu_buffer_rsrc_t wsr = __builtin_amdgcn_make_buffer_rsrc(ws, 0, WS_END, 0x00020000);
    const int hbase = 2 * ck - 8;
    const int nskip = hbase < 0 ? -hbase : 0;
    const int q4 = (lane & 15) >> 2;
    const int qb = isL ? 1 : 0;
    const unsigned myflag = WS_FLAG + (unsigned)(hg * 32 + ck) * 128 + wave * 4;
    const u32x4 sentv = {SENT, SENT, SENT, SENT};
    unsigned tagv;

    u32x4 mk0, mk1;
    u64 um[16];
    unsigned foff_h, foff_o;
    unsigned hvo, oslot, osrc;
    LAS char *kp, *vp0, *qp, *hchk, *wb, *rpw, *rpr;
    int vstr;
    u16* dst;
    auto prepare = [&]() AINL {
#pragma unroll
        for (int j = 0; j < 4; ++j) {
            const int ka = ((2 * j) & 3) + 8 * ((2 * j) >> 2) + 4 * hh;
            mk0[j] = ((ka > r32) ? 0xFFFFu : 0u) | ((ka + 1 > r32) ? 0xFFFF0000u : 0u);
            mk1[j] = ((ka + 16 > r32) ? 0xFFFFu : 0u) | ((ka + 17 > r32) ? 0xFFFF0000u : 0u);
        }
        PIN(mk0); PIN(mk1);
        const int l8 = lane & 7;
        const int pc = ck - 1 - (l8 >> 1);
        foff_h = WS_FLAG + (unsigned)(hg * 32 + (pc < 0 ? ck : pc)) * 128 + ((hd >> 1) * 2 + (l8 & 1)) * 4;
        foff_o = WS_FLAG + (unsigned)((2 * hd + (l8 >> 2)) * 32 + (orow >> 6)) * 128 + (qb * 4 + (l8 & 3)) * 4;
        hvo = (unsigned)((int)WS_K + ((hg * 4 + hd) * 64 + hbase) * 1024 + lane * 16);
        hchk = lds + L_K + hd * 10240 + lane * 16 + 12;
        osrc = WS_O + (unsigned)(((orow >> 5) + qb) * 32 + hd * 8) * 1024 + lane * 16;
        kp = lds + L_K + hd * 10240 + qb * 1024 + lane * 16;
        qp = lds + L_Q + hd * 2048 + qb * 1024 + lane * 16;
        vp0 = lds + ((r32 < 16) ? (L_V + hd * 10240 + qb * 1024 + hh * 256 + r32 * 16) : (L_ONES + qb * 256));
        vstr = (r32 < 16) ? 1024 : 16;
        wb = lds + hd * 16384 + (4 * hh + q4) * 128 + ((lane >> 4) & 1) * 32 + (lane & 3) * 8 + ((q4 >> 1) << 6);
        rpw = lds + L_RED + wave * 9216 + r32 * 144 + hh * 16;
        const int qr = hd * 8 + (lane >> 3), co = lane & 7;
        rpr = lds + L_RED + (wave & 4) * 9216 + (co >> 2) * 4608 + qr * 144 + (co & 3) * 32;
        dst = out + (size_t)(orow + qb * 32 + qr) * 512 + ocol + co * 8;
        PIN(foff_h); PIN(foff_o); PIN(hvo); PIN(hchk); PIN(osrc); PIN(kp); PIN(qp); PIN(vp0); PIN(vstr); PIN(wb); PIN(rpw); PIN(rpr); PIN(dst);
    };
    oslot = WS_O + (unsigned)((ck * 2 + qb) * 32 + hg * 4 + hd) * 1024 + lane * 16;
    auto make_um = [&]() AINL {
#pragma unroll
        for (int i = 0; i < 16; ++i) { um[i] = upper_mask(i); asm volatile("" : "+s"(um[i])); }
    };
    const unsigned hdst = lbase + L_K + hd * 10240;
    auto issue_halo = [&](int v) AINL {
#pragma unroll
        for (int i = 0; i < 8; ++i) blds16_sc1(wsr, hvo, i >= nskip ? i * 1024 + v * (WS_V - WS_K) : OOB, hdst + v * 40960 + i * 1024);
    };
    auto halo_probe = [&](int v) AINL -> bool {
        unsigned mx = 0;
        CFENCE();
#pragma unroll
        for (int t = 0; t < 8; ++t) mx = max(mx, *(LAS unsigned*)(hchk + v * 40960 + t * 1024));
        return __all(mx != SENT);
    };
    auto halo_check = [&](int v) AINL {
        if (UNLIKELY(!halo_probe(v))) {
            STAMP(28);
#pragma unroll 1
            for (int spin = 0; spin < SPIN_CAP; ++spin) {
#pragma unroll 1
                for (int i = 0; i < 8; ++i)
                    blds16_sc1(wsr, hvo, __builtin_amdgcn_readfirstlane(i >= nskip ? i * 1024 + v * (WS_V - WS_K) : OOB), __builtin_amdgcn_readfirstlane(hdst + v * 40960 + i * 1024));
                waitv<0>();
                if (halo_probe(v)) break;
            }
        }
    };
    auto flag_load = [&](unsigned off) AINL -> unsigned {
        unsigned f;
        asm volatile("buffer_load_dword %0, %1, %2, 0 offen sc1" : "=v"(f) : "v"(off), "s"(wsr) : "memory");
        return f;
    };
    auto flag_poll = [&](unsigned f, unsigned off) AINL {
        if (UNLIKELY(!__all(f == tagv))) {
#pragma unroll 1
            for (int spin = 0; spin < SPIN_CAP; ++spin) {
                __builtin_amdgcn_s_sleep(1);
                asm volatile("buffer_load_dword %0, %1, %2, 0 offen sc1\n\ts_waitcnt vmcnt(0)" : "=v"(f) : "v"(off), "s"(wsr) : "memory");
                if (__all(f == tagv)) break;
            }
        }
    };
    unsigned ocf = 0;
    u32x4 ov;
    bool ok;

    auto attention = [&](auto ISL) AINL {
        constexpr bool L = decltype(ISL)::value;
        u32x4 P[9][2];
        f32x16 cn, T0, T1, Sb, O;
        const f32x16 zero = {};
        const bf16x8 qf = *(LAS bf16x8*)qp;
        auto kread = [&](int t) AINL { return *(LAS bf16x8*)(kp + t * 1024); };
        bf16x8 vf[9][2], kf[9];
        auto vread = [&](int t) AINL { LAS char* vp = vp0 + t * vstr; vf[t][0] = *(LAS bf16x8*)(vp); vf[t][1] = *(LAS bf16x8*)(vp + 512); };
        auto pvh = [&](int t, int h) AINL { O = MFMA32(vf[t][h], as_bf(P[t][h]), O); };
        const u16* wop = wo + (size_t)(hd * 128) * 512 + ocol;
        const unsigned ovo = (unsigned)((lane >> 3) * 1024) + wchunk;
        auto issue_wo = [&](int i0) AINL {
            if constexpr (!L) {
#pragma unroll
                for (int i = i0; i < i0 + 4; ++i) glds16(wop + (size_t)i * 4096, ovo, lbase + hd * 16384 + i * 1024);
            }
        };
        auto exps = [&](int t, f32x16& T, int a0, int a1, int a2, int a3) AINL {
            const int act[4] = {a0, a1, a2, a3};
#pragma unroll
            for (int c = 0; c < 4; ++c) {
                if (act[c] >= 0) pvh(act[c] >> 1, act[c] & 1);
#pragma unroll
                for (int i = 4 * c; i < 4 * c + 4; ++i) T[i] = __builtin_amdgcn_exp2f(T[i]);
                if (c == 1) P[t][0] = pack8(T, 0);
                if (c == 3) P[t][1] = pack8(T, 8);
                SB();
            }
            PIN(P[t][0]); PIN(P[t][1]);
        };
        __builtin_amdgcn_s_setprio(L ? 3 : 2);
        {
            const bf16x8 k7 = kread(7);
            kf[6] = kread(6); kf[5] = kread(5);
            f32x16 S = MFMA32(k7, qf, zero);
            float m = fmaxf(S[0], S[1]);
#pragma unroll
            for (int i = 2; i < 16; i += 2) m = fmaxf(fmaxf(m, S[i]), S[i + 1]);
            const float nm = -(xhalf_max(m) + HEADROOM);
#pragma unroll
            for (int i = 0; i < 16; ++i) cn[i] = nm;
            T1 = MFMA32(k7, qf, cn);
        }
        T0 = MFMA32(kf[6], qf, cn);
        SB();
        exps(7, T1, -1, -1, -1, -1);
        T1 = MFMA32(kf[5], qf, cn); kf[4] = kread(4);
        SB();
        exps(6, T0, -1, -1, -1, -1);
        T0 = MFMA32(kf[4], qf, cn); kf[3] = kread(3);
        STAMP(13);
        if constexpr (L) {
            waitv<0>();
            __builtin_amdgcn_raw_buffer_store_b32(tagv, wsr, myflag, 0, 16);
            halo_check(1);
        }
        bar();
        STAMP(14);
        O = zero;
        vread(7); vread(6);
        issue_wo(0);
        SB();
        if constexpr (!L) __builtin_amdgcn_s_setprio(3);
        exps(5, T1, -1, 14, 15, 12);
        T1 = MFMA32(kf[3], qf, cn); kf[2] = kread(2);
        vread(5); vread(4);
        issue_wo(4);
        SB();
        __builtin_amdgcn_s_setprio(2);
        exps(4, T0, 13, -1, 10, 11);
        T0 = MFMA32(kf[2], qf, cn); kf[1] = kread(1);
        vread(3);
        issue_wo(8);
        SB();
        exps(3, T1, 8, -1, 9, -1);
        T1 = MFMA32(kf[1], qf, cn); kf[0] = kread(0); kf[8] = kread(8);
        vread(2);
        issue_wo(12);
        SB();
        __builtin_amdgcn_s_setprio(1);
        exps(2, T0, 6, -1, 7, -1);
        T0 = MFMA32(kf[0], qf, cn);
        Sb = MFMA32(kf[8], qf, cn);
        vread(1);
        ocf = flag_load(foff_o);
        SB();
        exps(1, T1, 4, -1, 5, -1);
        vread(0); vread(8);
        SB();
        __builtin_amdgcn_s_setprio(0);
#pragma unroll
        for (int c = 0; c < 4; ++c) {
            if (c < 2) pvh(1, c);
#pragma unroll
            for (int i = 4 * c; i < 4 * c + 4; ++i) T0[i] = __builtin_amdgcn_exp2f(sel(Sb[i], T0[i], um[i]));
            if (c == 1) { const u32x4 p0 = pack8(T0, 0); P[0][0] = p0 & mk0; P[8][0] = p0 ^ P[0][0]; }
            if (c == 2) { pvh(0, 0); pvh(8, 0); }
            if (c == 3) { const u32x4 p1 = pack8(T0, 8); P[0][1] = p1 & mk1; P[8][1] = p1 ^ P[0][1]; }
            SB();
        }
        pvh(0, 1); pvh(8, 1);
        const float l = O[8];
        const float rl = __builtin_amdgcn_rcpf(l);
        float chk = 0.f;
#pragma unroll
        for (int i = 0; i < 8; ++i) { O[i] *= rl; chk += O[i]; }
        ov = pack8(O, 0);
        ok = __all(l < 0x1p126f && l > 0x1p-100f && __builtin_fabsf(chk) < __builtin_inff());
    };

    if (isL) {
        unsigned tag, kfl = 0;
        asm volatile("s_load_dword %0, %1, 0x0" : "=s"(tag) : "s"(ws) : "memory");
        prepare();
        static_for<16>([&](auto NC) AINL {
            constexpr int n = decltype(NC)::value;
            constexpr int last = n == 0 ? 3 : n + 2 < 15 ? n + 2 : 15;
            waitv<4 * (last - n) + ((n > 9 && n <= 12) ? 1 : 0) + (n > KH_AT ? 8 : 0)>();
            __builtin_amdgcn_s_barrier();
            if (n == 0) STAMP(20);
            if (n >= 1 && n + 3 < 16) issue_step(n + 3);
            if (n == 9) kfl = flag_load(foff_h);
            if (n == KH_AT) {
                asm volatile("s_waitcnt lgkmcnt(0)" : "+s"(tag), "+v"(kfl) :: "memory");
                tagv = tag + 1u;
                flag_poll(kfl, foff_h);
                issue_halo(0);
            }
        });
        make_um();
        waitv<0>();
        issue_halo(1);
        __builtin_amdgcn_raw_buffer_store_b128(sentv, wsr, oslot, 0, 16);
        halo_check(0);
        STAMP(16);
        __builtin_amdgcn_s_barrier();
        STAMP(5);
        attention(IC<1>{});
    } else {
        const int ft = wave >> 1, tb = wave & 1;
        asm volatile("buffer_load_dword %0, off, %1, 0 sc1" : "=v"(tagv) : "s"(wsr) : "memory");
        const unsigned kb0 = (unsigned)((hg * 4 + 2 * ft) * 64 + ck * 2 + tb) * 1024;
        unsigned kdst = WS_K + kb0 + lane * 16;
        const int vsub = hh * 256 + (r32 & 15) * 16;
        unsigned vdst = WS_V + kb0 + (r32 >> 4) * 65536 + vsub;
        {
            const unsigned o1 = (lane & 15) + ((lane >> 4) & 1) >= nskip ? 0x3F803F80u : 0u;
            const u32x4 one = {o1, o1, o1, o1};
            *(LAS u32x4*)(lds + L_ONES + lane * 16) = one;
        }
        prepare();
        LAS char* xa[4];
        {
            const int u = hh ^ ((r32 >> 1) & 7);
#pragma unroll
            for (int ks = 0; ks < 4; ++ks) { xa[ks] = lds + L_RING + (tb * 32 + r32) * 128 + (((2 * ks) ^ u) << 4); PIN(xa[ks]); }
        }
        LAS char* wa0 = lds + (8 * hh + q4) * 128 + ((ft ^ (q4 >> 1)) << 6) + ((lane >> 4) & 1) * 32 + (lane & 3) * 8;
        LAS char* vl = lds + L_V + (2 * ft + (r32 >> 4)) * 10240 + (8 + tb) * 1024 + vsub;
        LAS char* kl = lds + L_K + (2 * ft) * 10240 + (8 + tb) * 1024 + lane * 16;
        LAS char* ql = lds + L_Q + (2 * ft) * 2048 + tb * 1024 + lane * 16;
        PIN(wa0); PIN(vdst); PIN(kdst); PIN(vl); PIN(kl); PIN(ql);
        auto wrd = [&](int off) AINL { return tr2(wa0 + off, wa0 + off + 512); };
        __builtin_amdgcn_raw_buffer_store_b128(sentv, wsr, kdst, 0, 16);
        __builtin_amdgcn_raw_buffer_store_b128(sentv, wsr, kdst, 65536, 16);
        __builtin_amdgcn_raw_buffer_store_b128(sentv, wsr, vdst, 0, 16);
        __builtin_amdgcn_raw_buffer_store_b128(sentv, wsr, vdst + 512, 0, 16);
        __builtin_amdgcn_raw_buffer_store_b128(sentv, wsr, oslot, 0, 16);
        SB();
        bf16x8 xf[32], wf[2][8];
        f32x16 acc = {};
        static_for<17>([&](auto NC) AINL {
            constexpr int n = decltype(NC)::value;
            constexpr int slot = slot_of(n);
            constexpr int p = n - 1;
            if (n < 16) {
                if (n >= 1 && n + 3 < 16) waitl0();
                __builtin_amdgcn_s_barrier();
                CFENCE();
                if (n % 4 == 0) STAMP(30 + n);
            }
            if (p == 8 || p == 12) acc = f32x16{};
            SB();
#pragma unroll
            for (int j = 0; j < 8; ++j) {
                if constexpr (p >= 0 && p < 8) { if (j < 4) acc = MFMA32(wf[p & 1][j], xf[4 * p + j], acc); }
                if constexpr (p >= 8 && p < 12) acc = MFMA32(xf[8 * (p - 8) + j], wf[p & 1][j], acc);
                if constexpr (p >= 12) acc = MFMA32(wf[p & 1][j], xf[8 * (p - 12) + j], acc);
                if constexpr (n < 8) if (j < 4) {
                    xf[4 * n + j] = *(LAS bf16x8*)(xa[j] + slot);
                    wf[n & 1][j] = wrd(slot + 8192 + j * 2048);
                }
                if (n >= 8 && n < 16 && j < 4) {
                    wf[n & 1][2 * j] = wrd(slot + j * 4096);
                    wf[n & 1][2 * j + 1] = wrd(slot + j * 4096 + 2048);
                }
                SB();
            }
            if (p == 7) {
                const u32x4 k0 = sanitize(pack8(acc, 0)), k1 = sanitize(pack8(acc, 8));
                __builtin_amdgcn_raw_buffer_store_b128(k0, wsr, kdst, 0, 16);
                __builtin_amdgcn_raw_buffer_store_b128(k1, wsr, kdst, 65536, 16);
                *(LAS u32x4*)(kl) = k0;
                *(LAS u32x4*)(kl + 10240) = k1;
                STAMP(2);
            }
            if (p == 11) {
                const u32x4 v0 = sanitize(pack8(acc, 0)), v1 = sanitize(pack8(acc, 8));
                __builtin_amdgcn_raw_buffer_store_b128(v0, wsr, vdst, 0, 16);
                __builtin_amdgcn_raw_buffer_store_b128(v1, wsr, vdst + 512, 0, 16);
                *(LAS u32x4*)(vl) = v0;
                *(LAS u32x4*)(vl + 512) = v1;
                STAMP(3);
            }
            if (n == AFLAG_POS) {
                asm volatile("s_waitcnt vmcnt(0)" : "+v"(tagv) :: "memory");
                tagv += 1u;
                __builtin_amdgcn_raw_buffer_store_b32(tagv, wsr, myflag, 0, 16);
            }
            if (n == 14) make_um();
        });
#pragma unroll
        for (int i = 0; i < 16; ++i) acc[i] *= CEXP;
        *(LAS u32x4*)(ql) = pack8(acc, 0);
        *(LAS u32x4*)(ql + 2048) = pack8(acc, 8);
        STAMP(4);
        bar();
        STAMP(5);
        attention(IC<0>{});
    }
    STAMP(25);
    if (UNLIKELY(!ok)) { STAMP(15); ov = unit_exact(lds, hd, qb, nskip, lane); }
    __builtin_amdgcn_raw_buffer_store_b128(sanitize(ov), wsr, oslot, 0, 16);
    STAMP(6);

    {
        asm volatile("s_waitcnt vmcnt(1)" : "+v"(ocf) :: "memory");
        bar();
        flag_poll(ocf, foff_o);
        u32x4 ef = {};
        if (INLINE_IF(bid == 0)) ef = __builtin_amdgcn_raw_buffer_load_b128(wsr, WS_FLAG + (unsigned)(wave * 32 + (lane >> 1)) * 128 + (lane & 1) * 16, 0, 16);
        CFENCE();
        bf16x8 wa[8][2];
        {
            LAS char* wb2 = lds + (int)((unsigned)(wb - lds) ^ 64u);
#pragma unroll
            for (int j = 0; j < 8; ++j) {
                wa[j][0] = tr2(wb + j * 2048, wb + j * 2048 + 1024);
                wa[j][1] = tr2(wb2 + j * 2048, wb2 + j * 2048 + 1024);
            }
        }
        u32x4 d[8];
        {
            const unsigned pofs = osrc - lane * 16 + (lane & 7) * 1024 + 1020;
            unsigned got = 0;
#pragma unroll 1
            for (int spin = 0; spin < SPIN_CAP; ++spin) {
                CFENCE();
                const unsigned pr = __builtin_amdgcn_raw_buffer_load_b32(wsr, pofs, 0, 16);
                const unsigned rdy = (unsigned)__builtin_amdgcn_ballot_w64(pr != SENT) & 0xFFu;
                const unsigned nw = rdy & ~got;
#pragma unroll
                for (int j = 0; j < 8; ++j)
                    if (nw >> j & 1) d[j] = __builtin_amdgcn_raw_buffer_load_b128(wsr, osrc + j * 1024, 0, 16);
                got |= rdy;
                if (got == 0xFFu) break;
                if (__builtin_popcount(got) >= 8 - DIRECT_TAIL) {
#pragma unroll 1
                    for (; spin < SPIN_CAP; ++spin) {
                        CFENCE();
#pragma unroll
                        for (int j = 0; j < 8; ++j)
                            if (!(got >> j & 1)) d[j] = __builtin_amdgcn_raw_buffer_load_b128(wsr, osrc + j * 1024, 0, 16);
#pragma unroll
                        for (int j = 0; j < 8; ++j)
                            if (!(got >> j & 1)) got |= __all(d[j].w != SENT) ? 1u << j : 0u;
                        if (got == 0xFFu) break;
                    }
                    break;
                }
                __builtin_amdgcn_s_sleep(PROBE_SLEEP);
            }
        }
        f32x16 c0 = {}, c1 = {};
        unsigned mx = 0;
#pragma unroll
        for (int j = 0; j < 8; ++j) {
            c0 = MFMA32(wa[j][0], as_bf(d[j]), c0);
            c1 = MFMA32(wa[j][1], as_bf(d[j]), c1);
            mx = max(mx, d[j].w);
        }
        if (UNLIKELY(!__all(mx != SENT))) {
#pragma unroll 1
            for (int spin = 0; spin < SPIN_CAP; ++spin) {
                bool pend = false;
                CFENCE();
#pragma unroll
                for (int j = 0; j < 8; ++j)
                    if (!__all(d[j].w != SENT)) { d[j] = __builtin_amdgcn_raw_buffer_load_b128(wsr, osrc + j * 1024, 0, 16); pend = true; }
                if (!pend) break;
            }
            c0 = f32x16{}; c1 = f32x16{};
#pragma unroll
            for (int j = 0; j < 8; ++j) {
                c0 = MFMA32(wa[j][0], as_bf(d[j]), c0);
                c1 = MFMA32(wa[j][1], as_bf(d[j]), c1);
            }
        }
        STAMP(8);
        if (INLINE_IF(bid == 0)) {
            const bool all = __all(ef.x == tagv && ef.y == tagv && ef.z == tagv && ef.w == tagv);
            *(volatile LAS unsigned*)(lds + L_MISC + wave * 4) = all ? 1u : 0u;
        }
#pragma unroll
        for (int gq = 0; gq < 4; ++gq) {
            f32x4 a = {c0[4 * gq], c0[4 * gq + 1], c0[4 * gq + 2], c0[4 * gq + 3]};
            f32x4 b = {c1[4 * gq], c1[4 * gq + 1], c1[4 * gq + 2], c1[4 * gq + 3]};
            *(LAS f32x4*)(rpw + gq * 32) = a;
            *(LAS f32x4*)(rpw + 4608 + gq * 32) = b;
        }
        bar();
        STAMP(9);
        {
            f32x4 a = *(LAS f32x4*)(rpr), b = *(LAS f32x4*)(rpr + 16);
#pragma unroll
            for (int s = 1; s < 4; ++s) { a += *(LAS f32x4*)(rpr + s * 9216); b += *(LAS f32x4*)(rpr + s * 9216 + 16); }
            const u32x4 r = {pack2(a[0], a[1]), pack2(a[2], a[3]), pack2(b[0], b[1]), pack2(b[2], b[3])};
            asm volatile("global_store_dwordx4 %0, %1, off sc1\nswa_code_tail:\n\ts_nop 1" :: "v"(dst), "v"(r) : "memory");
        }
        if (INLINE_IF(bid == 0 && wave == 7)) {
            if (UNLIKELY(!__all(*(volatile LAS unsigned*)(lds + L_MISC + (lane & 7) * 4) != 0))) {
#pragma unroll 1
                for (int spin = 0; spin < SPIN_CAP; ++spin) {
                    CFENCE();
                    bool all = true;
#pragma unroll
                    for (int i = 0; i < 8; ++i) {
                        const u32x4 f = __builtin_amdgcn_raw_buffer_load_b128(wsr, WS_FLAG + ((i >> 1) * 64 + lane) * 128 + (i & 1) * 16, 0, 16);
                        all &= f.x == tagv && f.y == tagv && f.z == tagv && f.w == tagv;
                    }
                    if (__all(all)) break;
                }
            }
            if (lane == 0) __builtin_amdgcn_raw_buffer_store_b32(tagv, wsr, WS_EPOCH, 0, 16);
        }
        STAMP(10); STAMP_FLUSH();
    }
}

extern "C" void kernel_launch(void* const* d_in, const int* in_sizes, int n_in, void* d_out, int out_size, void* d_ws,
                              size_t ws_size, hipStream_t stream) {
    (void)in_sizes; (void)n_in; (void)out_size; (void)ws_size;
    (void)hipFuncSetAttribute((const void*)swa_block, hipFuncAttributeMaxDynamicSharedMemorySize, LDS_BYTES);
    swa_block<<<256, 512, LDS_BYTES, stream>>>((const u16*)d_in[0], (const u16*)d_in[1], (const u16*)d_in[2], (u16*)d_out, (char*)d_ws);
}
```

```cpp
#include <hip/hip_runtime.h>
#include <stdint.h>
#include <type_traits>

typedef short bf16x8 __attribute__((ext_vector_type(8)));
typedef short s16x4 __attribute__((ext_vector_type(4)));
typedef float f32x16 __attribute__((ext_vector_type(16)));
typedef float f32x4 __attribute__((ext_vector_type(4)));
typedef float f32x2 __attribute__((ext_vector_type(2)));
typedef unsigned u32x4 __attribute__((ext_vector_type(4)));
typedef unsigned u32x2 __attribute__((ext_vector_type(2)));
typedef double f64x8 __attribute__((ext_vector_type(8)));
typedef __bf16 bf16x2 __attribute__((ext_vector_type(2)));
typedef unsigned short u16;
typedef unsigned long long u64;
#define LAS __attribute__((address_space(3)))
#define DI __device__ __forceinline__

constexpr int AFLAG_POS = 9;
constexpr int FETCH_PACE = 35;
constexpr int FETCH_EARLY = 5;

constexpr unsigned WS_EPOCH = 0;
constexpr unsigned WS_FLAG = 1024;
constexpr unsigned WS_K = 65536;
constexpr unsigned WS_V = WS_K + 32 * 64 * 1024;
constexpr unsigned WS_O = WS_V + 32 * 64 * 1024;
constexpr unsigned WS_END = WS_O + 64 * 32 * 1024;
constexpr int L_RING = 0;
constexpr int L_K = 65536;
constexpr int L_V = L_K + 40960;
constexpr int L_Q = L_V + 40960;
constexpr int L_ONES = L_Q + 8192;
constexpr int L_RED = L_K;
constexpr int L_MISC = L_ONES + 5120;
constexpr int L_TAG = L_MISC + 128;
constexpr int LDS_BYTES = L_MISC + 256;
constexpr int slot_of(int n) { return (n & 3) * 16384; }

constexpr float CEXP = 0.36067376022224085f;
constexpr float HEADROOM = 72.f;
constexpr unsigned SENT = 0xFFFFFFFFu;
constexpr unsigned OOB = 0x40000000u;
constexpr int SPIN_CAP = 1 << 18;

DI unsigned pack2(float a, float b) {
    f32x2 v = {a, b};
    return __builtin_bit_cast(unsigned, __builtin_convertvector(v, bf16x2));
}
DI u32x4 pack8(const f32x16& a, int o) {
    u32x4 r = {pack2(a[o], a[o + 1]), pack2(a[o + 2], a[o + 3]), pack2(a[o + 4], a[o + 5]), pack2(a[o + 6], a[o + 7])};
    return r;
}
DI u32x4 sanitize(u32x4 v) {
    v.w = min(v.w, SENT - 1);
    return v;
}
DI bf16x8 as_bf(u32x4 v) { return __builtin_bit_cast(bf16x8, v); }
DI void waitl0() { asm volatile("" ::: "memory"); __builtin_amdgcn_s_waitcnt(0xC07F); asm volatile("" ::: "memory"); }
DI void bar() { waitl0(); __builtin_amdgcn_s_barrier(); asm volatile("" ::: "memory"); }
template <int N> DI void waitv() { asm volatile("s_waitcnt vmcnt(%0)" :: "i"(N) : "memory"); }
#define CFENCE() asm volatile("" ::: "memory")
#define SB() __builtin_amdgcn_sched_barrier(0)
#define PIN(x) asm volatile("" : "+v"(x))
#define MFMA32(a, b, c) __builtin_amdgcn_mfma_f32_32x32x16_bf16((a), (b), (c), 0, 0, 0)
DI s16x4 trread(LAS char* p) { return __builtin_amdgcn_ds_read_tr16_b64_v4i16((LAS s16x4*)p); }
DI bf16x8 tr2(LAS char* p0, LAS char* p1) {
    s16x4 a = trread(p0), b = trread(p1);
    return __builtin_shufflevector(a, b, 0, 1, 2, 3, 4, 5, 6, 7);
}
DI void glds16x2(const void* sbase, unsigned v0, unsigned v1, unsigned ldsaddr) {
    asm volatile("s_mov_b32 m0, %0\n\ts_nop 0\n\t"
                 "global_load_lds_dwordx4 %1, %3\n\t"
                 "global_load_lds_dwordx4 %2, %3 offset:1024"
                 :: "s"(ldsaddr), "v"(v0), "v"(v1), "s"(sbase) : "memory");
}
DI void glds16x4(const void* sbase, unsigned v0, unsigned v1, unsigned v2, unsigned v3, unsigned ldsaddr) {
    asm volatile("s_mov_b32 m0, %0\n\ts_nop 0\n\t"
                 "global_load_lds_dwordx4 %1, %5\n\t"
                 "global_load_lds_dwordx4 %2, %5 offset:1024\n\t"
                 "global_load_lds_dwordx4 %3, %5 offset:2048\n\t"
                 "global_load_lds_dwordx4 %4, %5 offset:3072"
                 :: "s"(ldsaddr), "v"(v0), "v"(v1), "v"(v2), "v"(v3), "s"(sbase) : "memory");
}
DI float xhalf_max(float m) {
    auto r = __builtin_amdgcn_permlane32_swap(__float_as_uint(m), __float_as_uint(m), false, false);
    return fmaxf(__uint_as_float(r[0]), __uint_as_float(r[1]));
}
#define UNLIKELY(c) __builtin_expect(!!(c), 0)
#define INLINE_IF(c) __builtin_expect(!!(c), 1)
#define AINL __attribute__((always_inline))
template <int V> using IC = std::integral_constant<int, V>;
template <int N, int I = 0, class F> DI void static_for(F&& f) { if constexpr (I < N) { f(IC<I>{}); static_for<N, I + 1>(f); } }
constexpr u64 upper_mask(int i) {
    u64 m = 0;
    for (int l = 0; l < 64; ++l) if ((i & 3) + 8 * (i >> 2) + 4 * (l >> 5) > (l & 31)) m |= 1ull << l;
    return m;
}
DI float sel(float lo, float hi, u64 m) { return __builtin_amdgcn_inverse_ballot_w64(m) ? hi : lo; }

DI void scale_ops(int lane, u32x4& sa, u32x4& sb) {
    const int d0 = ((lane >> 1) & 1) + ((lane >> 2) & 2) + (((lane >> 2) ^ (lane >> 5)) & 1) * 4 + (lane & 16);
    const unsigned m = 0xFFFFu << (16 * (lane & 1));
#pragma unroll
    for (int d = 0; d < 4; ++d) { const unsigned md = d0 == d ? m : 0u; sa[d] = md & 0x3EE33EE3u; sb[d] = md & 0xBA2EBA2Eu; }
}

DI u32x4 unit_exact(LAS char* lds, int hd, int qb, int nskip, int lane) {
    const int r32 = lane & 31, hh = lane >> 5;
    u32x4 sa, sb;
    scale_ops(lane, sa, sb);
    const bf16x8 qf = *(LAS bf16x8*)(lds + L_Q + hd * 2048 + qb * 1024 + lane * 16);
    const bf16x8 qg = as_bf(pack8(MFMA32(as_bf(sa), qf, MFMA32(as_bf(sb), qf, f32x16{})), 0));
    LAS char* kp = lds + L_K + hd * 10240 + qb * 1024 + lane * 16;
    const int va0 = (r32 < 16) ? (L_V + hd * 10240 + qb * 1024 + hh * 256 + r32 * 16) : (L_ONES + qb * 16);
    const int vup = (r32 < 16) ? 0 : 32 - 5120;
    const float NINF = -__builtin_inff();
    const int t0 = nskip > qb ? nskip - qb : 0;
    const f32x16 zero = {};
    float m = NINF;
#pragma unroll 1
    for (int t = t0; t < 9; ++t) {
        const bf16x8 kf = *(LAS bf16x8*)(kp + t * 1024);
        f32x16 S = MFMA32(kf, qf, MFMA32(kf, qg, zero));
#pragma unroll
        for (int i = 0; i < 16; ++i) {
            const int key = (i & 3) + 8 * (i >> 2) + 4 * hh;
            const bool ok = t == 0 ? key > r32 : t == 8 ? key <= r32 : true;
            m = fmaxf(m, ok ? S[i] : NINF);
        }
    }
    m = xhalf_max(m);
    f32x16 O = {};
#pragma unroll 1
    for (int t = t0; t < 9; ++t) {
        const bf16x8 kf = *(LAS bf16x8*)(kp + t * 1024);
        f32x16 S = MFMA32(kf, qf, MFMA32(kf, qg, zero));
#pragma unroll
        for (int i = 0; i < 16; ++i) {
            const int key = (i & 3) + 8 * (i >> 2) + 4 * hh;
            const bool ok = t == 0 ? key > r32 : t == 8 ? key <= r32 : true;
            S[i] = ok ? __builtin_amdgcn_exp2f(S[i] - m) : 0.f;
        }
        LAS char* vp = lds + va0 + t * 1024 + (t >= 5 ? vup : 0);
        O = MFMA32(*(LAS bf16x8*)(vp), as_bf(pack8(S, 0)), O);
        O = MFMA32(*(LAS bf16x8*)(vp + 512), as_bf(pack8(S, 8)), O);
    }
    const float rl = __builtin_amdgcn_rcpf(O[8]);
#pragma unroll
    for (int i = 0; i < 8; ++i) O[i] *= rl;
    return pack8(O, 0);
}

__global__ __launch_bounds__(512, 2) void swa_block(const u16* __restrict__ x, const u16* __restrict__ wqkv,
                                                    const u16* __restrict__ wo, u16* __restrict__ out, char* ws) {
    extern __shared__ __attribute__((aligned(16))) char smem_[];
    LAS char* lds = (LAS char*)smem_;
    {
        unsigned off;
        asm volatile("v_cmp_gt_u32 vcc, 4, %2\n\t"
                     "s_cbranch_vccz 1f\n\t"
                     "s_getpc_b64 s[90:91]\n"
                     "swa_code_head:\n\t"
                     "s_lshl_b32 s92, %3, 4\n\t"
                     "s_andn2_b32 s92, s92, 0x7f\n\t"
                     "v_lshl_add_u32 %0, %2, 12, s92\n\t"
                     "v_min_u32 %0, swa_code_tail-swa_code_head, %0\n\t"
                     "s_mov_b32 m0, %1\n\t"
                     "s_and_saveexec_b64 s[92:93], vcc\n\t"
                     "global_load_lds_dword %0, s[90:91]\n\t"
                     "s_mov_b64 exec, s[92:93]\n"
                     "1:"
                     : "=&v"(off) : "s"((unsigned)(uintptr_t)lds + L_MISC), "v"(threadIdx.x), "s"(blockIdx.x) : "memory", "vcc", "scc", "s90", "s91", "s92", "s93");
    }
    SB();
    const int tid = threadIdx.x;
    const int wave = __builtin_amdgcn_readfirstlane(tid >> 6);
    const int lane = tid & 63, r32 = lane & 31, hh = lane >> 5;
    const int bid = blockIdx.x, g = bid & 7, ii = bid >> 3;
    const bool two = !(g & 1);
    const int hg = two ? (g & 2) + (ii & 1) : 4 + (ii & 3);
    const int ck = two ? (g >> 2) * 16 + (ii >> 1) : (g >> 1) * 8 + (ii >> 2);
    const int rot = (two ? (ii >> 1) + 4 * (ii & 1) : (ii >> 2) + 2 * (ii & 3)) & 7;
    const unsigned lbase = (unsigned)(uintptr_t)lds;
    const int hd = wave & 3;
    const bool isL = wave >= 4;
    const unsigned wchunk = (unsigned)(((((lane >> 2) & 1) ^ ((lane >> 4) & 1)) * 4 + (lane & 3)) * 16);
    const unsigned prow = (unsigned)(((tid >> 2) & 48) + (lane >> 3));
    unsigned vx0 = (unsigned)(ck * 65536) + prow * 1024 + (((lane & 7) ^ (lane >> 4)) << 4), vx1 = (vx0 ^ 64u) + 7168;
    unsigned vw0 = prow * 3072 + (unsigned)(hg * 128) + wchunk, vw1 = vw0 + 23552;
    auto issue_w = [&](int s, int col, unsigned d) AINL { glds16x2((const char*)wqkv + (unsigned)(((s + rot) & 7) * 196608 + col * 2), vw0, vw1, d); };
    auto issue_step = [&](int n) AINL {
        const unsigned d = lbase + L_RING + slot_of(n) + hd * 2048;
        if (n < 8) {
            glds16x2((const char*)x + (unsigned)(((n + rot) & 7) * 128), vx0, vx1, d);
            issue_w(n, 512, d + 8192);
        } else {
            issue_w(2 * (n & 3), n < 12 ? 1024 : 0, d);
            issue_w(2 * (n & 3) + 1, n < 12 ? 1024 : 0, d + 8192);
        }
    };
    u32x4 sa, sb;
    if (__builtin_expect(isL, 1)) {
        PIN(vx0); PIN(vx1); PIN(vw0); PIN(vw1);
        SB();
        issue_step(0);
        asm volatile("" :: "s"(wo), "s"(out), "s"(ws));
        issue_step(1); issue_step(2); issue_step(3);
    }
    { SB(); int ln = lane; PIN(ln); scale_ops(ln, sa, sb); PIN(sa); PIN(sb); }
    const int orow = g * 256 + (ii >> 3) * 64, ocol = (ii & 7) * 64;
    const __amdgpu_buffer_rsrc_t wsr = __builtin_amdgcn_make_buffer_rsrc(ws, 0, WS_END, 0x00020000);
    const int hbase = 2 * ck - 8;
    const int nskip = hbase < 0 ? -hbase : 0;
    const int q4 = (lane & 15) >> 2;
    const int qb = isL ? 1 : 0;
    const unsigned myflag = WS_FLAG + (unsigned)(hg * 32 + ck) * 128 + wave * 4;
    const u32x4 sentv = {SENT, SENT, SENT, SENT};
    unsigned tagv;

    u32x4 mk0, mk1;
    u64 um[16];
    unsigned foff_h, foff_o;
    unsigned hv[4], oslot, osrc;
    LAS char *kp, *vp0, *vp5, *qp, *hchk, *wb, *rpw, *rpr;
    u16* dst;
    unsigned ov0, ov1, ov2, ov3;
    float hroom;
    auto prepare = [&](auto ISL) AINL {
        int ln = lane; PIN(ln);
        const int r32 = ln & 31, hh = ln >> 5, q4 = (ln & 15) >> 2, l8 = ln & 7;
        if constexpr (decltype(ISL)::value) {
            const int pc = ck - 1 - (l8 >> 1);
            foff_h = WS_FLAG + (unsigned)(hg * 32 + (pc < 0 ? ck : pc)) * 128 + ((hd >> 1) * 2 + (l8 & 1)) * 4;
#pragma unroll
            for (int j = 0; j < 4; ++j) {
                hv[j] = 2 * j >= nskip ? (unsigned)((int)WS_K + ((hg * 4 + hd) * 64 + hbase) * 1024 + ln * 16 + (j >> 1) * 4096) : OOB;
                PIN(hv[j]);
            }
            hchk = lds + L_V + hd * 10240 + ln * 16 + 8;
            PIN(foff_h); PIN(hchk);
        } else {
            ov0 = (unsigned)((ln >> 3) * 1024) + wchunk; ov1 = ov0 + 7168; ov2 = ov0 + 14336; ov3 = ov0 + 21504;
            PIN(ov0); PIN(ov1); PIN(ov2); PIN(ov3);
        }
        hroom = !decltype(ISL)::value && ck == 0 ? -8.f : -HEADROOM;
        PIN(hroom);
        foff_o = WS_FLAG + (unsigned)((2 * hd + (l8 >> 2)) * 32 + (orow >> 6)) * 128 + 16 + (l8 & 3) * 8;
        oslot = WS_O + (unsigned)((ck * 2 + qb) * 32 + hg * 4 + hd) * 1024 + ln * 16;
        osrc = WS_O + (unsigned)(((orow >> 5) + qb) * 32 + hd * 8) * 1024 + ln * 16;
        kp = lds + L_K + hd * 10240 + qb * 1024 + ln * 16;
        qp = lds + L_Q + hd * 2048 + qb * 1024 + ln * 16;
        const int oz = (r32 & 11) == 0 ? (r32 & 4) * 16 + qb * 16 : ((r32 + 4) & 8) ? 128 : 192;
        vp0 = lds + ((r32 < 16) ? (L_V + hd * 10240 + qb * 1024 + hh * 256 + r32 * 16) : L_ONES + oz);
        vp5 = vp0 + ((r32 < 16) ? 5120 : 32);
        wb = lds + hd * 16384 + (4 * hh + q4) * 128 + ((ln >> 4) & 1) * 32 + (ln & 3) * 8 + ((q4 >> 1) << 6);
        const int qr = hd * 8 + (ln >> 3);
        rpw = lds + L_RED + wave * 8448 + r32 * 128 + ((hh ^ (r32 & 7)) << 4);
        rpr = lds + L_RED + (wave & 4) * 8448 + (l8 >> 2) * 4224 + qr * 128 + ((((l8 & 3) * 2) ^ (qr & 7)) << 4);
        dst = out + (size_t)(orow + qb * 32 + qr) * 512 + ocol + l8 * 8;
        PIN(foff_o); PIN(oslot); PIN(osrc); PIN(kp); PIN(qp); PIN(vp0); PIN(vp5); PIN(wb); PIN(rpw); PIN(rpr); PIN(dst);
    };
    auto make_masks = [&]() AINL {
#pragma unroll
        for (int i = 0; i < 16; ++i) { um[i] = upper_mask(i); asm volatile("" : "+s"(um[i])); }
#pragma unroll
        for (int j = 0; j < 4; ++j) {
            const unsigned a = __builtin_amdgcn_inverse_ballot_w64(um[2 * j + 1]) ? 0xFFFF0000u : 0u;
            const unsigned b = __builtin_amdgcn_inverse_ballot_w64(um[2 * j + 9]) ? 0xFFFF0000u : 0u;
            mk0[j] = __builtin_amdgcn_inverse_ballot_w64(um[2 * j]) ? 0xFFFFFFFFu : a;
            mk1[j] = __builtin_amdgcn_inverse_ballot_w64(um[2 * j + 8]) ? 0xFFFFFFFFu : b;
        }
        PIN(mk0); PIN(mk1);
    };
    const unsigned hdst = lbase + L_K + hd * 10240;
    auto issue_halo = [&](int v) AINL {
#pragma unroll
        for (int h = 0; h < 2; ++h)
            asm volatile("s_mov_b32 m0, %0\n\ts_nop 0\n\t"
                         "buffer_load_dwordx4 %1, %3, %4 offen sc1 lds\n\t"
                         "buffer_load_dwordx4 %1, %3, %4 offen offset:1024 sc1 lds\n\t"
                         "buffer_load_dwordx4 %2, %3, %4 offen offset:2048 sc1 lds\n\t"
                         "buffer_load_dwordx4 %2, %3, %4 offen offset:3072 sc1 lds"
                         :: "s"(hdst + v * 40960 + h * 4096), "v"(hv[2 * h]), "v"(hv[2 * h + 1]), "s"(wsr), "s"(v * (WS_V - WS_K)) : "memory");
    };
    auto vhalo_ok = [&]() AINL -> bool {
        u32x2 w[8];
#pragma unroll
        for (int t = 0; t < 8; ++t) asm volatile("ds_read_b64 %0, %1 offset:%2" : "=v"(w[t]) : "v"(hchk), "i"(t * 1024) : "memory");
        asm volatile("s_waitcnt lgkmcnt(0)" : "+v"(w[0]), "+v"(w[1]), "+v"(w[2]), "+v"(w[3]), "+v"(w[4]), "+v"(w[5]), "+v"(w[6]), "+v"(w[7]) :: "memory");
        unsigned mx = 0;
#pragma unroll
        for (int t = 0; t < 8; ++t) mx = max(mx, w[t].y);
        return __all(mx != SENT);
    };
    u32x2 ocf;
    u32x4 ef;
    unsigned tbv, tend;
    bool ok;

    auto efload = [&]() AINL {
        unsigned t;
        asm volatile("s_cmp_lg_u32 %4, 0\n\ts_cbranch_scc1 1f\n\tv_lshlrev_b32 %1, 6, %2\n\tv_and_b32 %1, 0x7f80, %1\n\t"
                     "buffer_load_dwordx4 %0, %1, %3, 0 offen offset:%5 sc1\n1:"
                     : "=v"(ef), "=&v"(t) : "v"(tid), "s"(wsr), "s"(bid), "i"(WS_FLAG) : "scc", "memory");
    };

    auto attention = [&](auto ISL) AINL {
        constexpr bool L = decltype(ISL)::value;
        u32x4 P[9][2];
        f32x16 cn, T[9], O;
        const f32x16 zero = {};
        bf16x8 qg;
        bf16x8 vf[9][2], kf[9];
        auto kread = [&](int t) AINL { kf[t] = *(LAS bf16x8*)(kp + t * 1024); };
        if constexpr (L) {
            CFENCE();
            kread(7); kread(6); kread(5);
            __builtin_amdgcn_s_setprio(3);
            CFENCE(); __builtin_amdgcn_s_barrier(); CFENCE();
        }
        const bf16x8 qf = *(LAS bf16x8*)qp;
        auto vread = [&](int t) AINL { LAS char* vp = t < 5 ? vp0 + t * 1024 : vp5 + (t - 5) * 1024; vf[t][0] = *(LAS bf16x8*)(vp); vf[t][1] = *(LAS bf16x8*)(vp + 512); };
        auto pvh = [&](int t, int h) AINL { O = MFMA32(vf[t][h], as_bf(P[t][h]), O); };
        auto qk0 = [&](int t) AINL { T[t] = MFMA32(kf[t], qf, cn); };
        auto qk1 = [&](int t) AINL { T[t] = MFMA32(kf[t], qg, T[t]); };
        const u16* wop = wo + (size_t)(hd * 128) * 512 + ocol;
        auto issue_wo = [&](int i0) AINL {
            if constexpr (!L) {
                SB();
                glds16x4(wop + (size_t)i0 * 4096, ov0, ov1, ov2, ov3, lbase + hd * 16384 + i0 * 1024);
            }
        };
        auto exps = [&](int t, int a0, int a1, int a2, int a3) AINL {
            const int act[4] = {a0, a1, a2, a3};
#pragma unroll
            for (int c = 0; c < 4; ++c) {
                if (act[c] >= 30) qk1(act[c] - 30); else if (act[c] >= 20) qk0(act[c] - 20); else if (act[c] >= 0) pvh(act[c] >> 1, act[c] & 1);
#pragma unroll
                for (int i = 4 * c; i < 4 * c + 4; ++i) T[t][i] = __builtin_amdgcn_exp2f(T[t][i]);
                if (c == 1) P[t][0] = pack8(T[t], 0);
                if (c == 3) P[t][1] = pack8(T[t], 8);
                SB();
            }
            PIN(P[t][0]); PIN(P[t][1]);
        };
        if constexpr (!L) __builtin_amdgcn_s_setprio(2);
        {
            if constexpr (!L) { kread(7); kread(6); kread(5); }
            f32x16 S = MFMA32(kf[7], qf, zero);
            PIN(S);
            if constexpr (!L) {
                SB();
                __builtin_amdgcn_raw_buffer_store_b128(sentv, wsr, oslot, 0, 16);
                __builtin_amdgcn_raw_buffer_store_b128(sentv, wsr, oslot, 32768, 16);
            }
            SB();
            float nm = __builtin_amdgcn_fmed3f(S[0], S[1], 3.0e38f);
            PIN(nm); SB();
            f32x16 qs = MFMA32(as_bf(sb), qf, zero);
            PIN(qs); SB();
#pragma unroll
            for (int i = 2; i < 8; i += 2) {
                nm = fmaxf(fmaxf(nm, S[i]), S[i + 1]);
                if (i == 4) { PIN(nm); SB(); qs = MFMA32(as_bf(sa), qf, qs); PIN(qs); SB(); }
            }
            nm = __builtin_fmaf(nm, -4.f * CEXP, hroom);
            {
                auto r = __builtin_amdgcn_permlane32_swap(__float_as_uint(nm), __float_as_uint(nm), false, false);
                f32x2 n2 = {__builtin_amdgcn_fmed3f(__uint_as_float(r[0]), __uint_as_float(r[1]), -3.0e38f), 0.f};
                n2[1] = n2[0];
                f64x8 c8;
                c8[0] = __builtin_bit_cast(double, n2);
#pragma unroll
                for (int i = 1; i < 8; ++i) { double e; asm volatile("v_mov_b64 %0, %1" : "=v"(e) : "v"(c8[0])); c8[i] = e; }
                asm volatile("s_nop 1" : "+v"(c8));
                cn = __builtin_bit_cast(f32x16, c8);
            }
            qk0(7);
            PIN(T[7]);
            SB();
            qg = as_bf(pack8(qs, 0));
            qk1(7);
            PIN(T[7]);
        }
        SB();
        exps(7, -1, 26, 36, -1);
        qk0(5); kread(4);
        u64 tq;
        SB();
        if constexpr (!L) asm volatile("s_memrealtime %0" : "=s"(tq));
        exps(6, -1, 35, -1, -1);
        qk0(4);
        SB();
        if constexpr (!L) asm volatile("s_waitcnt lgkmcnt(0)" : "+s"(tq));
        kread(3);
        if constexpr (L) {
#pragma unroll 1
            for (int spin = 0; spin < SPIN_CAP; ++spin) {
                waitv<0>();
                if (vhalo_ok()) break;
                issue_halo(1);
            }
        }
        if constexpr (!L) {
            waitv<0>();
            const u32x2 f = {tagv, (unsigned)tq};
            __builtin_amdgcn_raw_buffer_store_b64(f, wsr, myflag + 16 + wave * 4, 0, 16);
        }
        CFENCE(); __builtin_amdgcn_s_barrier(); CFENCE();
        asm volatile("s_memrealtime %0" : "=s"(tq));
        O = zero;
        vread(7); vread(6);
        issue_wo(0);
        SB();
        if constexpr (!L) __builtin_amdgcn_s_setprio(3);
        exps(5, 34, 14, 15, 12);
        asm volatile("s_waitcnt lgkmcnt(0)" : "+s"(tq));
        tbv = (unsigned)tq - 16u;
        qk0(3); kread(2);
        vread(5); vread(4);
        issue_wo(4);
        SB();
        __builtin_amdgcn_s_setprio(2);
        exps(4, 13, 33, 10, 11);
        qk0(2); kread(1);
        vread(3);
        issue_wo(8);
        SB();
        exps(3, 8, 32, 9, -1);
        qk0(1); kread(0); kread(8);
        vread(2);
        issue_wo(12);
        SB();
        __builtin_amdgcn_s_setprio(1);
        exps(2, 6, 31, 7, 28);
        qk0(0);
        vread(1);
        asm volatile("buffer_load_dwordx2 %0, %1, %2, 0 offen sc1" : "=v"(ocf) : "v"(foff_o), "s"(wsr) : "memory");
        efload();
        SB();
        exps(1, 38, 30, 4, 5);
        vread(0); vread(8);
        SB();
        if constexpr (L) __builtin_amdgcn_s_setprio(0);
#pragma unroll
        for (int c = 0; c < 4; ++c) {
            if (c < 2) pvh(1, c);
#pragma unroll
            for (int i = 4 * c; i < 4 * c + 4; ++i) T[0][i] = __builtin_amdgcn_exp2f(sel(T[8][i], T[0][i], um[i]));
            if (c == 1) { const u32x4 p0 = pack8(T[0], 0); P[0][0] = p0 & mk0; P[8][0] = p0 ^ P[0][0]; }
            if (c == 2) { pvh(0, 0); pvh(8, 0); }
            if (c == 1) asm volatile("s_memrealtime %0" : "=s"(tq));
            if (c == 3) { const u32x4 p1 = pack8(T[0], 8); P[0][1] = p1 & mk1; P[8][1] = p1 ^ P[0][1]; }
            SB();
        }
        pvh(0, 1); pvh(8, 1);
        const float l = O[8];
        const float rl = __builtin_amdgcn_rcpf(l);
        f32x2 o2[4];
#pragma unroll
        for (int i = 0; i < 4; ++i) o2[i] = f32x2{O[2 * i], O[2 * i + 1]};
        const f32x2 c2 = (o2[0] + o2[1]) + (o2[2] + o2[3]);
#pragma unroll
        for (int i = 0; i < 8; ++i) O[i] *= rl;
        u32x4 ov = pack8(O, 0);
        ok = (l < 0x1p126f) & (l > 0x1p-100f) & (__builtin_fabsf(c2[0] + c2[1]) < __builtin_inff());
        ov.w = ok ? min(ov.w, SENT - 1) : SENT;
        __builtin_amdgcn_raw_buffer_store_b128(ov, wsr, oslot, 0, 16);
        asm volatile("s_waitcnt lgkmcnt(0)" : "+s"(tq));
        asm volatile("s_waitcnt vmcnt(1)" : "+v"(ocf), "+v"(ef) :: "memory");
        tend = (unsigned)tq + 18u;
    };

    if (isL) {
        unsigned kfl = 0;
        static_for<13>([&](auto NC) AINL {
            constexpr int n = decltype(NC)::value;
            waitv<(n == 0 ? 12 : 8) + (n == 12 ? 1 : 0)>();
            __builtin_amdgcn_s_barrier();
            if (n >= 1) issue_step(n + 3);
            if (n == 9) { SB(); prepare(IC<1>{}); SB(); }
            if (n == 11) {
                __builtin_amdgcn_s_sleep(2);
                unsigned f;
                asm volatile("buffer_load_dword %0, %1, %2, 0 offen sc1" : "=v"(f) : "v"(foff_h + 0), "s"(wsr) : "memory");
                kfl = f;
            }
            if (n == 12) tagv = *(volatile LAS unsigned*)(lds + L_TAG);
        });
        waitv<5>();
        __builtin_amdgcn_s_barrier();
        asm volatile("s_waitcnt vmcnt(4)" : "+v"(kfl) :: "memory");
#pragma unroll 1
        for (int spin = 0; spin < SPIN_CAP; ++spin) {
            if (__all(kfl == tagv)) break;
            asm volatile("buffer_load_dword %0, %1, %2, 0 offen sc1\n\ts_waitcnt vmcnt(0)" : "=v"(kfl) : "v"(foff_h), "s"(wsr) : "memory");
        }
        issue_halo(0);
        waitv<8>();
        __builtin_amdgcn_s_barrier();
        SB();
        make_masks();
        waitv<0>();
        issue_halo(1);
        attention(IC<1>{});
    } else {
        const int ft = wave >> 1, tb = wave & 1;
        unsigned sd = 0;
        {
            const int idx = bid * 4 + wave;
            const char* pw = (const char*)wqkv + (idx & 511) * 3072 + (idx >> 9 ? 0 : 2048) + ((3 - (int)((uintptr_t)wqkv >> 7)) & 7) * 128;
            const char* po = (const char*)wo + (idx >> 1) * 1024 + ((3 - (int)((uintptr_t)wo >> 7)) & 7) * 128 + (idx & 1) * 64;
            asm volatile("s_load_dword %0, %1, 0\n\ts_load_dword %0, %1, 64\n\ts_load_dword %0, %2, 0" : "+s"(sd) : "s"(pw), "s"(po) : "memory");
        }
        const unsigned kb0 = (unsigned)((hg * 4 + 2 * ft) * 64 + ck * 2 + tb) * 1024;
        unsigned kdst = WS_K + kb0 + lane * 16;
        const int vsub = hh * 256 + (r32 & 15) * 16;
        unsigned vdst = WS_V + kb0 + (r32 >> 4) * 65536 + vsub;
        {
            const int row = min(wave * 4 + (lane >> 4), 9);
            const unsigned o1 = (row >> 1) + 5 * ((lane >> 1) & 1) + (lane & 1) >= nskip && !(lane & 8) ? 0x3F803F80u : 0u;
            const u32x4 one = {o1, o1, o1, o1};
            *(LAS u32x4*)(lds + L_ONES + row * 512 + (lane & 15) * 16) = one;
        }
        prepare(IC<0>{});
        make_masks();
        LAS char* xa[4];
        {
            const int u = hh ^ ((r32 >> 1) & 7);
#pragma unroll
            for (int ks = 0; ks < 4; ++ks) { xa[ks] = lds + L_RING + (tb * 32 + r32) * 128 + (((2 * ks) ^ u) << 4); PIN(xa[ks]); }
        }
        LAS char* wa0 = lds + (8 * hh + q4) * 128 + ((ft ^ (q4 >> 1)) << 6) + ((lane >> 4) & 1) * 32 + (lane & 3) * 8;
        LAS char* vl = lds + L_V + (2 * ft + (r32 >> 4)) * 10240 + (8 + tb) * 1024 + vsub;
        LAS char* kl = lds + L_K + (2 * ft) * 10240 + (8 + tb) * 1024 + lane * 16;
        LAS char* ql = lds + L_Q + (2 * ft) * 2048 + tb * 1024 + lane * 16;
        PIN(wa0); PIN(vdst); PIN(kdst); PIN(vl); PIN(kl); PIN(ql);
        auto wrd = [&](int off) AINL { return tr2(wa0 + off, wa0 + off + 512); };
        __builtin_amdgcn_s_sleep(5);
        __builtin_amdgcn_raw_buffer_store_b128(sentv, wsr, vdst, 0, 16);
        __builtin_amdgcn_raw_buffer_store_b128(sentv, wsr, vdst + 512, 0, 16);
        SB();
        bf16x8 xf[32], wf[2][8];
        f32x16 acc = {};
        u32x4 v0, v1;
        static_for<17>([&](auto NC) AINL {
            constexpr int n = decltype(NC)::value;
            constexpr int slot = slot_of(n);
            constexpr int p = n - 1;
            constexpr int em = n == 8 ? 1 : n == 12 ? 2 : n == 13 ? 3 : 0;
            auto sync = [&]() AINL {
                if (n >= 1 && n + 3 < 16 && em == 0) waitl0();
                CFENCE();
                __builtin_amdgcn_s_barrier();
                CFENCE();
            };
            s16x4 wh[16];
            auto reads = [&](int j) AINL {
                if constexpr (n < 8) if (j < 4) {
                    xf[4 * n + j] = *(LAS bf16x8*)(xa[j] + slot);
                    wf[n & 1][j] = wrd(slot + 8192 + j * 2048);
                }
                if constexpr (n >= 8 && n < 16) {
                    if (n >= 14) wf[n & 1][j] = wrd(slot + j * 2048);
                    else if (em == 0) {
                        constexpr int lo[9] = {0, 3, 6, 9, 12, 14, 16, 16, 16};
#pragma unroll
                        for (int h = lo[j]; h < lo[j + 1]; ++h) wh[h] = trread(wa0 + slot + (h >> 1) * 2048 + (h & 1) * 512);
                        if (j == 7) {
#pragma unroll
                            for (int h = 0; h < 8; ++h) wf[n & 1][h] = __builtin_shufflevector(wh[2 * h], wh[2 * h + 1], 0, 1, 2, 3, 4, 5, 6, 7);
                        }
                    } else if (j < 4) {
                        wf[n & 1][2 * j] = wrd(slot + j * 4096);
                        wf[n & 1][2 * j + 1] = wrd(slot + j * 4096 + 2048);
                    }
                }
            };
            auto sync_reads = [&]() AINL {
                SB();
                sync();
                SB();
#pragma unroll
                for (int j = 0; j < 4; ++j) reads(j);
                SB();
            };
            if (n == 2) asm volatile("s_waitcnt lgkmcnt(0)" : "+s"(sd));
            if (n < 16 && n != 14 && em == 0) sync();
            if (p == 8 || p == 12) acc = f32x16{};
            SB();
#pragma unroll
            for (int j = 0; j < 8; ++j) {
                if constexpr (p >= 0 && p < 8) { if (j < 4) acc = MFMA32(wf[p & 1][j], xf[4 * p + j], acc); }
                if constexpr (p >= 8 && p < 12) acc = MFMA32(xf[8 * (p - 8) + j], wf[p & 1][j], acc);
                if constexpr (p >= 12) acc = MFMA32(wf[p & 1][j], xf[8 * (p - 12) + j], acc);
                SB();
                if (em == 0) reads(j);
                if (em == 3) {
                    if (j == 0) *(LAS u32x4*)(vl) = v0;
                    if (j == 1) *(LAS u32x4*)(vl + 512) = v1;
                    if (j == 3) { SB(); sync(); SB(); }
                    if (j >= 3 && j < 7) reads(j - 3);
                }
                SB();
            }
            if (em == 2) sync_reads();
            if (p == 7) {
                const u32x4 k0 = pack8(acc, 0), k1 = pack8(acc, 8);
                __builtin_amdgcn_raw_buffer_store_b128(k0, wsr, kdst, 0, 16);
                __builtin_amdgcn_raw_buffer_store_b128(k1, wsr, kdst, 65536, 16);
                asm volatile("buffer_load_dword %0, off, %1, 0 sc1" : "=v"(tagv) : "s"(wsr) : "memory");
                sync_reads();
                *(LAS u32x4*)(kl) = k0;
                *(LAS u32x4*)(kl + 10240) = k1;
            }
            if (p == 11) {
                v0 = sanitize(pack8(acc, 0)); v1 = sanitize(pack8(acc, 8));
                __builtin_amdgcn_raw_buffer_store_b128(v0, wsr, vdst, 0, 16);
                __builtin_amdgcn_raw_buffer_store_b128(v1, wsr, vdst + 512, 0, 16);
            }
            if (n == AFLAG_POS) {
                asm volatile("s_waitcnt vmcnt(0)" : "+v"(tagv) :: "memory");
                tagv += 1u;
                __builtin_amdgcn_raw_buffer_store_b32(tagv, wsr, myflag, 0, 16);
                *(volatile LAS unsigned*)(lds + L_TAG) = tagv;
            }
        });
        acc *= 0.25f;
        *(LAS u32x4*)(ql) = pack8(acc, 0);
        *(LAS u32x4*)(ql + 2048) = pack8(acc, 8);
        bar();
        attention(IC<0>{});
    }
    if (UNLIKELY(!__all(ok))) {
        const u32x4 ov = unit_exact(lds, hd, qb, nskip, lane);
        waitv<0>();
        __builtin_amdgcn_raw_buffer_store_b128(sanitize(ov), wsr, ok ? OOB : oslot, 0, 16);
    }

    {
        __builtin_amdgcn_s_setprio(0);
        if (UNLIKELY(!__all(ocf.x == tagv))) {
#pragma unroll 1
            for (int spin = 0; spin < SPIN_CAP; ++spin) {
                __builtin_amdgcn_s_sleep(1);
                asm volatile("buffer_load_dwordx2 %0, %1, %2, 0 offen sc1\n\ts_waitcnt vmcnt(0)" : "=v"(ocf) : "v"(foff_o), "s"(wsr) : "memory");
                if (__all(ocf.x == tagv)) break;
            }
        }
        const int need = max((int)(__builtin_amdgcn_readlane(ocf.y, 0) - tbv), (int)(__builtin_amdgcn_readlane(ocf.y, 4) - tbv)) + FETCH_PACE;
        int early = ((need - FETCH_EARLY - 1) & (wave - 4)) >> 31;
        asm volatile("" : "+s"(early));
        if (!early) {
            bar();
            if (need > 10) {
                int rem = min((int)(tend + (unsigned)need - (unsigned)__builtin_amdgcn_s_memrealtime()), 150);
                asm volatile("s_cmp_lt_i32 %0, 3\n\ts_cbranch_scc1 2f\n1:\n\ts_sleep 1\n\ts_sub_i32 %0, %0, 4\n\ts_cmp_gt_i32 %0, 2\n\ts_cbranch_scc1 1b\n2:" : "+s"(rem) :: "scc");
            }
        }
        const unsigned osrc2 = osrc + 4096;
        LAS char *rpx[4], *rpr2;
        u32x4 d[8];
        bf16x8 wa[8][2];
        f32x16 c0, c1 = {};
        auto put = [&](const f32x16& c, int ch, int gq) AINL {
            const f32x4 a = {c[4 * gq], c[4 * gq + 1], c[4 * gq + 2], c[4 * gq + 3]};
            *(LAS f32x4*)(rpx[gq] + ch * 4224) = a;
        };
#pragma unroll 1
        for (int spin = 0; spin < SPIN_CAP; ++spin) {
#pragma unroll
            for (int j = 0; j < 8; ++j)
                asm volatile("buffer_load_dwordx4 %0, %1, %2, 0 offen offset:%3 sc1" : "=v"(d[j]) : "v"(j < 4 ? osrc : osrc2), "s"(wsr), "i"((j & 3) * 1024) : "memory");
            PIN(wb); PIN(rpr); PIN(rpw);
            LAS char* wb2 = lds + (int)((unsigned)(wb - lds) ^ 64u);
#pragma unroll
            for (int j = 0; j < 8; ++j) {
                wa[j][0] = tr2(wb + j * 2048, wb + j * 2048 + 1024);
                wa[j][1] = tr2(wb2 + j * 2048, wb2 + j * 2048 + 1024);
            }
            rpr2 = lds + (int)((unsigned)(rpr - lds) ^ 16u);
#pragma unroll
            for (int gq = 0; gq < 4; ++gq) { rpx[gq] = lds + (int)((unsigned)(rpw - lds) ^ (unsigned)(gq * 32)); PIN(rpx[gq]); }
            PIN(rpr2);
            SB();
            asm volatile("s_cmp_eq_u32 %0, 0\n\ts_cbranch_scc1 1f\n\ts_barrier\n\ts_mov_b32 %0, 0\n1:" : "+s"(early) :: "scc", "memory");
            unsigned mx = 0;
            c0 = f32x16{};
            static_for<8>([&](auto JC) AINL {
                constexpr int j = decltype(JC)::value;
                asm volatile("s_waitcnt vmcnt(%1)" : "+v"(d[j]) : "i"(7 - j) : "memory");
                c0 = MFMA32(wa[j][0], as_bf(d[j]), c0);
                mx = max(mx, d[j].w);
                SB();
            });
            if (__all(mx != SENT)) break;
        }
#pragma unroll
        for (int j = 0; j < 8; ++j) {
            c1 = MFMA32(wa[j][1], as_bf(d[j]), c1);
            if (j >= 2 && j < 6) put(c0, 0, j - 2);
            SB();
        }
        if (INLINE_IF(bid == 0)) {
            const bool all = __all(ef.x == tagv && ef.y == tagv && ef.z == tagv && ef.w == tagv);
            *(volatile LAS unsigned*)(lds + L_MISC + wave * 4) = all ? 1u : 0u;
        }
#pragma unroll
        for (int gq = 0; gq < 4; ++gq) put(c1, 1, gq);
        bar();
        {
            f32x4 pa[4], pb[4];
#pragma unroll
            for (int s = 0; s < 4; ++s) { pa[s] = *(LAS f32x4*)(rpr + s * 8448); pb[s] = *(LAS f32x4*)(rpr2 + s * 8448); }
            SB();
            const f32x4 a = (pa[0] + pa[1]) + (pa[2] + pa[3]), b = (pb[0] + pb[1]) + (pb[2] + pb[3]);
            const u32x4 r = {pack2(a[0], a[1]), pack2(a[2], a[3]), pack2(b[0], b[1]), pack2(b[2], b[3])};
            asm volatile("global_store_dwordx4 %0, %1, off sc1\nswa_code_tail:\n\ts_nop 1" :: "v"(dst), "v"(r) : "memory");
        }
        if (INLINE_IF(bid == 0 && wave == 7)) {
            if (UNLIKELY(!__all(*(volatile LAS unsigned*)(lds + L_MISC + (lane & 7) * 4) != 0))) {
#pragma unroll 1
                for (int spin = 0; spin < SPIN_CAP; ++spin) {
                    CFENCE();
                    bool all = true;
#pragma unroll
                    for (int i = 0; i < 4; ++i) {
                        const u32x4 f = __builtin_amdgcn_raw_buffer_load_b128(wsr, WS_FLAG + (i * 64 + lane) * 128, 0, 16);
                        all &= f.x == tagv && f.y == tagv && f.z == tagv && f.w == tagv;
                    }
                    if (__all(all)) break;
                }
            }
            if (lane == 0) __builtin_amdgcn_raw_buffer_store_b32(tagv, wsr, WS_EPOCH, 0, 16);
        }
    }
}

extern "C" void kernel_launch(void* const* d_in, const int* in_sizes, int n_in, void* d_out, int out_size, void* d_ws,
                              size_t ws_size, hipStream_t stream) {
    (void)in_sizes; (void)n_in; (void)out_size; (void)ws_size;
    (void)hipFuncSetAttribute((const void*)swa_block, hipFuncAttributeMaxDynamicSharedMemorySize, LDS_BYTES);
    swa_block<<<256, 512, LDS_BYTES, stream>>>((const u16*)d_in[0], (const u16*)d_in[1], (const u16*)d_in[2], (u16*)d_out, (char*)d_ws);
}
```
